```python
import torch

def _softmax(x, axis=-1):
    return torch.softmax(x, dim=axis)

def _gelu(x):
    return torch.nn.functional.gelu(x, approximate='tanh')

def _silu(x):
    return torch.nn.functional.silu(x)

def _sigmoid(x):
    return torch.sigmoid(x)

def _layernorm(x, axis=-1, eps=1e-05):
    m = x.mean(dim=axis, keepdim=True)
    v = x.var(dim=axis, keepdim=True, unbiased=False)
    return (x - m) / torch.sqrt(v + eps)

def _scatter_add(tgt, idx, src, axis=0):
    return tgt.clone().index_add_(axis, idx, src)

def _segment_sum(src, seg, n):
    out = torch.zeros((n,) + src.shape[1:], dtype=src.dtype)
    return out.index_add_(0, seg, src)

def _pad(x, pw):
    flat = tuple((v for pair in reversed(pw) for v in pair))
    return torch.nn.functional.pad(x, flat)
kernel_name = 'adaLN_cond__sliding_block_cached__glu_gated_glu__decode'

def setup_inputs():

    _w = lambda a: (lambda t: t.to(torch.bfloat16) if t.is_floating_point() else t)(torch.as_tensor(a))

    def _A_setup():
        B, Sq, D = (8, 1, 512)
        Hq, Hkv, Sk, Dh = (32, 32, 4096, 128)
        assert Hq % Hkv == 0, (Hq, Hkv)
        return {'x': _w((lambda *s: torch.randn(*s, dtype=torch.bfloat16) * 0.05)(B, Sq, D)), 'k_cache': _w((lambda *s: torch.randn(*s, dtype=torch.bfloat16) * 3.2)(B, Hkv, Sk, Dh)), 'v_cache': _w((lambda *s: torch.randn(*s, dtype=torch.bfloat16) * 0.4)(B, Hkv, Sk, Dh)), 'wq': _w((lambda *s: torch.randn(*s, dtype=torch.bfloat16) * 3.2)(D, Hq * Dh) * D ** (-0.5)), 'wo': _w((lambda *s: torch.randn(*s, dtype=torch.bfloat16) * 0.4)(Hq * Dh, D) * (Hq * Dh) ** (-0.5))}
    _A_in = _A_setup()

    def _B_setup():
        B, S, D, F = (8, 1, 512, 1376)
        return {'x': _w((lambda *s: torch.randn(*s, dtype=torch.bfloat16) * 0.05)(B, S, D)), 'w_gate': _w((lambda *s: torch.randn(*s, dtype=torch.bfloat16) * 0.05)(D, F)), 'w_up': _w((lambda *s: torch.randn(*s, dtype=torch.bfloat16) * 0.05)(D, F)), 'w_down': _w((lambda *s: torch.randn(*s, dtype=torch.bfloat16) * 0.05)(F, D))}
    _B_in = _B_setup()
    return {'A_x': _A_in['x'], 'A_k_cache': _A_in['k_cache'], 'A_v_cache': _A_in['v_cache'], 'A_wq': _A_in['wq'], 'A_wo': _A_in['wo'], 'B_x': _B_in['x'], 'B_w_gate': _B_in['w_gate'], 'B_w_up': _B_in['w_up'], 'B_w_down': _B_in['w_down'], 'c': _w((lambda *s: torch.randn(*s, dtype=torch.bfloat16) * 0.1)(8, 512) * 8.0), 'w_mod': _w((lambda *s: torch.randn(*s, dtype=torch.bfloat16) * 0.1)(512, 6 * 512) * 512 ** (-0.5) * 8.0)}

def reference(A_x, A_k_cache, A_v_cache, A_wq, A_wo, B_x, B_w_gate, B_w_up, B_w_down, c, w_mod):

    def _A_ref(x, k_cache, v_cache, wq, wo):
        B, Sq, D = x.shape
        Hq, Dh = (32, 128)
        Hkv, Sk = (k_cache.shape[1], k_cache.shape[2])
        W = min(4096, max(256, Sk // 4))
        rep = Hq // Hkv
        q = (lambda x, *d: x.permute(*d))((x @ wq).reshape(B, Sq, Hq, Dh), 0, 2, 1, 3)
        k = (lambda x, n, axis: torch.repeat_interleave(x, n, dim=axis))(k_cache[:, :, Sk - W:, :], rep, axis=1)
        v = (lambda x, n, axis: torch.repeat_interleave(x, n, dim=axis))(v_cache[:, :, Sk - W:, :], rep, axis=1)
        scores = torch.einsum('bhqd,bhkd->bhqk', q, k) * Dh ** (-0.5)
        p = _softmax(scores, axis=-1)
        o = torch.einsum('bhqk,bhkd->bhqd', p, v)
        return (lambda x, *d: x.permute(*d))(o, 0, 2, 1, 3).reshape(B, Sq, Hq * Dh) @ wo

    def _B_ref(x, w_gate, w_up, w_down):
        g = torch.einsum('bsd,df->bsf', x, w_gate)
        u = torch.einsum('bsd,df->bsf', x, w_up)
        h = _sigmoid(g) * u
        return torch.einsum('bsf,fd->bsd', h, w_down)
    x = A_x
    mod = (_silu(c) @ w_mod).reshape(c.shape[0], 6, 1, -1)
    sa, ca, ga, sb, cb, gb = (mod[:, 0].clone(), mod[:, 1].clone(), mod[:, 2].clone(), mod[:, 3].clone(), mod[:, 4].clone(), mod[:, 5].clone())
    h = _layernorm(x, axis=-1) * (1 + ca) + sa
    x = x + ga * _A_ref(h, A_k_cache, A_v_cache, A_wq, A_wo)
    h = _layernorm(x, axis=-1) * (1 + cb) + sb
    x = x + gb * _B_ref(h, B_w_gate, B_w_up, B_w_down)
    return x
```

```cpp
#include <hip/hip_runtime.h>
#include <hip/hip_bf16.h>
#include <stdint.h>

typedef unsigned short u16;
typedef unsigned int u32;
typedef unsigned long long u64;
typedef __attribute__((ext_vector_type(2))) unsigned int u32x2;
typedef __attribute__((ext_vector_type(4))) unsigned int u32x4;
typedef __attribute__((ext_vector_type(2))) float f32x2;
typedef __attribute__((ext_vector_type(4))) float f32x4;
typedef __attribute__((ext_vector_type(2))) __bf16 bf16x2_t;
typedef __attribute__((ext_vector_type(8))) __bf16 bf16x8_t;
typedef __attribute__((ext_vector_type(16))) __bf16 bf16x16_t;
typedef __attribute__((ext_vector_type(8))) unsigned int u32x8;
typedef __attribute__((ext_vector_type(16))) unsigned int u32x16;
typedef __attribute__((ext_vector_type(4))) short s16x4;
#define GAS __attribute__((address_space(1)))
#define LAS __attribute__((address_space(3)))
#define RLX_AGENT __ATOMIC_RELAXED, __HIP_MEMORY_SCOPE_AGENT
#define RLX_WG __ATOMIC_RELAXED, __HIP_MEMORY_SCOPE_WORKGROUP
#define SB() __builtin_amdgcn_sched_barrier(0)
#define MEMFENCE() asm volatile("" ::: "memory")
#define PIN(x) asm volatile("" : "+v"(x))

constexpr int B = 8, D = 512, H = 32, SK = 4096, DH = 128, W = 1024, F = 1376, NMOD = 6 * D;

#define PFS 8
#define NPRE 24
#define NQ 8
#define NSM 3
#define PD 9
#define RMP 4
#define NPAUSE 14
#define NPAUSEQ 18
#define NPAUSEM 20
#define PWA 3
#define NWP 14
#define NWP0 8
#define NWM 14
#define NWM0 6
#define NQA 3
#define NWQ 8
#define PB0 3
#define PB1 5
#define PB2 8
#define TQP 850
#define THP 580
#define TAILPF 61
#define B2B 0
#define PSJ 25
#define NWE 8
#define EPW 239
#define NPROBE 1
#define D5 44
#define JD0 22
#define TSTAR 1930
#define TAU 25
#define PK0 36
#define PKS 2
#define D2 4

#define CT 1
#define PLK 4
#define PLV 4
#define PLP 6
#define K32 430
#define K64 940
#define K88 1280
#define K99 1445
#define K104 1545
#define K128 1860
#define KSH 0
#define NOPUB 0
#define RDREF 1
#define CCODE 1
#define CEV 4
#define HG 0
#define TROFS 75
#define UOFS 15
#define NOFS 15
#define A2 0
#define AI 80
#define AR 2
#define AMIN 3
#define PSH 0
#define UOFP 0
#define NOFP 0
#define CPV 1
#define CVP 1
#define RAL 8
#define RI 2
#define SWT 5
#define JS 3
constexpr int WS_EPOCH = 0;
constexpr int WS_RF = 8192;
constexpr int WS_HG = 16384;
constexpr int WS_MG = 32768;
constexpr int WS_QG = 1 << 20;
constexpr int WS_SPAN = 2 << 20;
constexpr int WS_TAIL = 4 << 20;
constexpr int T_IN = 0;
constexpr int T_X1 = 512 << 10;
constexpr int T_PR = 768 << 10;
constexpr int T_CP = 800 << 10;
constexpr int T_INX = 1 << 20;
__device__ __forceinline__ int q_off(int b, int h) { return WS_QG + (b * 2048 + h * 64) * 8; }
__device__ __forceinline__ int mg_off(int b, int k) { return WS_MG + (b * 4 + k) * 2048; }
__device__ __forceinline__ int cls(int x) { return (((x >> 1) & 3) | ((x & 1) << 2)) << 12; }
__device__ __forceinline__ int x1_off(int r, int b) { return T_X1 + (b >> 1) * 32768 + (cls(r) ^ 16384) + (b & 1) * 2048; }
__device__ __forceinline__ int in_off(int b, int rc) { return T_IN + (b * 2 + (rc >> 4)) * 32768 + cls(rc) + ((rc >> 3) & 1) * 2048; }
__device__ __forceinline__ int inx_off(int b, int rc) { return T_INX + b * 32768 + (cls(rc) ^ 4096) + (rc >> 3) * 1024; }
constexpr size_t WS_STAMPS = 8 << 20;
constexpr unsigned SPIN_CAP = 1u << 20;
constexpr int OOB = 0x7ffffff0;

constexpr int L_K = 0;
constexpr int L_MP = 131072;
constexpr int L_PT = L_MP + 16384;
constexpr int L_Q = L_PT + 16 * 68 * 4;
constexpr int L_ML = L_Q + 256;
constexpr int L_OP = L_ML + 64;
constexpr int L_H2 = L_MP;
constexpr int HROW = 1088, OROW = 320;
constexpr int L_HH = L_OP + 5120;
constexpr int L_FL = L_HH + 512;
constexpr int L_ST = L_FL + 128;
constexpr int L_PG = L_ST + 128;
constexpr int L_END = L_PG + 1024;
static_assert(L_END <= 163840, "LDS");

__device__ __forceinline__ float lo2f(u32 x) { return __uint_as_float(x << 16); }
__device__ __forceinline__ float hi2f(u32 x) { return __uint_as_float(x & 0xffff0000u); }
__device__ __forceinline__ u32 cvtpk(float lo, float hi) {
  const bf16x2_t v = __builtin_convertvector(f32x2{lo, hi}, bf16x2_t);
  return __builtin_bit_cast(u32, v);
}
__device__ __forceinline__ float rbf(float x) { return lo2f(cvtpk(x, x)); }
__device__ __forceinline__ float dot2(u32 a, u32 b, float acc) {
  return __builtin_amdgcn_fdot2_f32_bf16(__builtin_bit_cast(bf16x2_t, a), __builtin_bit_cast(bf16x2_t, b), acc, false);
}
__device__ __forceinline__ f32x4 mfma(u32x4 a, u32x4 b, f32x4 c) {
  return __builtin_amdgcn_mfma_f32_16x16x32_bf16(__builtin_bit_cast(bf16x8_t, a), __builtin_bit_cast(bf16x8_t, b), c, 0, 0, 0);
}
__device__ __forceinline__ f32x4 smfmac(u32x4 a, u32x8 b, f32x4 c, int idx) {
  return __builtin_amdgcn_smfmac_f32_16x16x64_bf16(__builtin_bit_cast(bf16x8_t, a), __builtin_bit_cast(bf16x16_t, b), c, idx, 0, 0);
}
__device__ __forceinline__ f32x2 fold(f32x4 v) { return f32x2{v[0], v[1]} + f32x2{v[2], v[3]}; }
__device__ __forceinline__ u32x8 cat(u32x4 a, u32x4 b) { return __builtin_shufflevector(a, b, 0, 1, 2, 3, 4, 5, 6, 7); }
template <int CTRL> __device__ __forceinline__ float dppf(float x) {
  return __builtin_bit_cast(float, __builtin_amdgcn_mov_dpp(__builtin_bit_cast(int, x), CTRL, 0xf, 0xf, true));
}
constexpr int XOR1 = 0xB1, XOR2 = 0x4E, XOR7 = 0x141, XOR15 = 0x140, XOR8 = 0x128;
__device__ __forceinline__ float swap32_add(float a, float b) {
  auto s = __builtin_amdgcn_permlane32_swap(__float_as_uint(a), __float_as_uint(b), false, false);
  return __uint_as_float(s[0]) + __uint_as_float(s[1]);
}
__device__ __forceinline__ float swap16_add(float a, float b) {
  auto s = __builtin_amdgcn_permlane16_swap(__float_as_uint(a), __float_as_uint(b), false, false);
  return __uint_as_float(s[0]) + __uint_as_float(s[1]);
}
__device__ __forceinline__ float half_allsum(float x) {
  x += dppf<XOR1>(x); x += dppf<XOR2>(x); x += dppf<XOR7>(x); x += dppf<XOR15>(x);
  return swap16_add(x, x);
}
__device__ __forceinline__ float wave_allsum(float x) { x = half_allsum(x); return swap32_add(x, x); }
__device__ __forceinline__ float wave_allmax(float x) {
  x = fmaxf(x, dppf<XOR1>(x)); x = fmaxf(x, dppf<XOR2>(x)); x = fmaxf(x, dppf<XOR7>(x)); x = fmaxf(x, dppf<XOR15>(x));
  auto s = __builtin_amdgcn_permlane16_swap(__float_as_uint(x), __float_as_uint(x), false, false);
  x = fmaxf(__uint_as_float(s[0]), __uint_as_float(s[1]));
  auto u = __builtin_amdgcn_permlane32_swap(__float_as_uint(x), __float_as_uint(x), false, false);
  return fmaxf(__uint_as_float(u[0]), __uint_as_float(u[1]));
}
template <int CTRL, int BIT> __device__ __forceinline__ float tr_row(float lo, float hi, int lane) {
  const float t1 = lo + dppf<CTRL>(lo), t2 = hi + dppf<CTRL>(hi);
  return (lane & BIT) ? t2 : t1;
}
__device__ __forceinline__ float row_tr_reduce16(float (&x)[16], int lane) {
#pragma unroll
  for (int i = 0; i < 8; ++i) x[i] = tr_row<XOR8, 8>(x[i], x[i + 8], lane);
#pragma unroll
  for (int i = 0; i < 4; ++i) x[i] = tr_row<XOR7, 4>(x[i], x[i + 4], lane);
#pragma unroll
  for (int i = 0; i < 2; ++i) x[i] = tr_row<XOR2, 2>(x[i], x[i + 2], lane);
  return tr_row<XOR1, 1>(x[0], x[1], lane);
}
__device__ __forceinline__ u32 perm_lo(u32 a, u32 b) { return __builtin_amdgcn_perm(b, a, 0x05040100u); }
__device__ __forceinline__ u32 perm_hi(u32 a, u32 b) { return __builtin_amdgcn_perm(b, a, 0x07060302u); }
__device__ __forceinline__ void to_frags(u32x4* L) {
  u32x4 o[8];
#pragma unroll
  for (int c = 0; c < 8; ++c)
#pragma unroll
    for (int j = 0; j < 4; ++j) o[c][j] = (c & 1) ? perm_hi(L[2 * j][c >> 1], L[2 * j + 1][c >> 1]) : perm_lo(L[2 * j][c >> 1], L[2 * j + 1][c >> 1]);
#pragma unroll
  for (int c = 0; c < 8; ++c) { L[c] = o[c]; PIN(L[c]); }
}
__device__ __forceinline__ void to_frags2(const u32x4* L, u32x8* o) {
#pragma unroll
  for (int c = 0; c < 8; ++c) {
#pragma unroll
    for (int j = 0; j < 8; ++j) o[c][j] = (c & 1) ? perm_hi(L[2 * j][c >> 1], L[2 * j + 1][c >> 1]) : perm_lo(L[2 * j][c >> 1], L[2 * j + 1][c >> 1]);
  }
#pragma unroll
  for (int c = 0; c < 8; ++c) PIN(o[c]);
}
__device__ __forceinline__ float sel4(float a0, float a1, float a2, float a3, int g) {
  const float x = (g & 1) ? a1 : a0, y = (g & 1) ? a3 : a2;
  return (g & 2) ? y : x;
}
__device__ __forceinline__ u32 ne3(u32 a, u32 b, u32 c) { return __builtin_amdgcn_bitop3_b32(a, b, c, 0x7E); }
__device__ __forceinline__ bool ok2(u32x4 v, u32 th) { return (v[1] == th) & (v[3] == th); }

#define BLD(rs, vo, so, AUX) __builtin_amdgcn_raw_buffer_load_b128((rs), (vo), (so), (AUX))
#define BLD64(rs, vo, so, AUX) __builtin_amdgcn_raw_buffer_load_b64((rs), (vo), (so), (AUX))
#define BLD32(rs, vo, so, AUX) __builtin_amdgcn_raw_buffer_load_b32((rs), (vo), (so), (AUX))
#define PUBLISH(pay, rs, vo, so) __builtin_amdgcn_raw_buffer_store_b64((u32x2{(pay), th}), (rs), (vo), (so), 16)
#define MKRS(p, n) __builtin_amdgcn_make_buffer_rsrc((void*)(p), 0, (n), 0x00020000)
#define VMWAIT(n) asm volatile("s_waitcnt vmcnt(%0)" :: "i"(n) : "memory")
#define BARRIER() do { asm volatile("s_waitcnt lgkmcnt(0)\n\ts_barrier" ::: "memory"); SB(); } while (0)
#define LDSWAIT() asm volatile("s_waitcnt lgkmcnt(0)" ::: "memory")
#define LADDR(off) ((u32)(uintptr_t)(LAS unsigned char*)lds + (u32)(off))
#define LPTR(T, a) ((LAS T*)(uintptr_t)(a))
#define STAMP(k) do {} while (0)
#define STAMPM(k) do {} while (0)
#define STAMP_DUMP() do {} while (0)
#define REF_STAMP() do {} while (0)
#define REF_STAMP2() do {} while (0)
#define CSTAMP2(k) do {} while (0)
#define CSTAMP(k) STAMPM(k)
#define EXIT() do { STAMP_DUMP(); return; } while (0)
#define POLL_BEGIN() { unsigned spin_ = SPIN_CAP; u64 more_; _Pragma("nounroll") do { u64 lim_; asm volatile("s_sub_u32 %0, %0, 1\n\ts_cselect_b64 %1, 0, -1" : "+s"(spin_), "=s"(lim_) :: "memory", "scc");
#define POLL_END(ok) more_ = __builtin_amdgcn_ballot_w64(!(ok)) & lim_; } while (more_ != 0ull); }
#define LDS_SPIN(ptr, n) do { for (unsigned spin_ = 0; __hip_atomic_load((ptr), RLX_WG) < (n) && spin_ < (SPIN_CAP << 3); ++spin_) __builtin_amdgcn_s_sleep(1); MEMFENCE(); } while (0)
#define LDS_ARRIVE(ptr) do { LDSWAIT(); if (lane == 0) __hip_atomic_fetch_add((ptr), 1u, RLX_WG); } while (0)

struct Args {
  const u16* x; const u16* kc; const u16* vc; const u16* wq; const u16* wo; const u16* c; const u16* wmod; unsigned char* ws;
  const u16* wg; const u16* wu; const u16* wd; u16* out;
};

constexpr int N1 = NPRE + NQ, N2 = N1 + NSM;
constexpr int issued(int i) { return i < 0 ? N2 : ((N2 + i / RMP > i + PD + 1 ? N2 + i / RMP : i + PD + 1) > 64 ? 64 : (N2 + i / RMP > i + PD + 1 ? N2 + i / RMP : i + PD + 1)); }
static_assert(NWP0 <= NWP && NWM <= NWP && NWM0 <= NWP && NWQ <= NWP && NWE <= NWP && NQA <= NWQ, "");
static_assert(NWP <= JD0 && JD0 >= 16 && JD0 <= 28 && JD0 % 2 == 0, "");
static_assert((EPW & 63) == 47, "EPW: neither an MLP unit nor a producer");
constexpr int sched(int i) { return KSH + (i < 64 ? K32 + (K64 - K32) * (i - 32) / 32 : i < 88 ? K64 + (K88 - K64) * (i - 64) / 24 : i < 99 ? K88 + (K99 - K88) * (i - 88) / 11 : i < 104 ? K99 + (K104 - K99) * (i - 99) / 5 : K104 + (K128 - K104) * (i - 104) / 24); }
#define WAIT_T(dt) do { while ((int)((u32)__builtin_amdgcn_s_memrealtime() - t0) < (dt)) __builtin_amdgcn_s_sleep(2); } while (0)
constexpr float SCALE = 0.08838834764831845f;

#define PADA 0x300
#define PADB 0xe04
#define PADC 0x5c
__device__ __attribute__((used, noinline, aligned(4096))) void k_filler() { asm volatile(".fill %0, 4, 0xBF800000" :: "i"(PADA / 4 - 16)); }

__global__ void __launch_bounds__(512, 2) k_block(Args a) {
  __shared__ __attribute__((aligned(16))) unsigned char lds[L_END];
  const int w = blockIdx.x, t = threadIdx.x;
  const int wave = __builtin_amdgcn_readfirstlane(t >> 6), lane = t & 63, g = lane >> 4, n = lane & 15;
  const int xcd = w & 7, jj = w >> 3;
  u32 pfc = 0;
  {
    u64 pc; int len;
    asm volatile("s_getpc_b64 %0\n.Lkb_pf_base:\n\ts_mov_b32 %1, .Lkb_pf_end-.Lkb_pf_base+128" : "=s"(pc), "=s"(len));
    const int lo = (jj * 9 + min(lane, 8)) * 128;
    if (wave == 1) pfc = *reinterpret_cast<const GAS u32*>(pc + (lo < len ? lo : 0));
    SB();
  }
  const int b = jj & 7, hg = jj >> 3;
  const int h = 4 * xcd + (hg ^ (xcd & 3)), bd = b ^ xcd;
  const size_t kvoff = ((size_t)(bd * H + h) * SK + (SK - W)) * DH;
  int vo_dma = (wave & 3) * 1024 + g * 256 + ((n ^ (4 * (wave & 3) + g)) << 4);
  const u32 l0 = LADDR(L_K + (wave & 3) * 1024);
  int vo = (wave & 3) * 1024 + lane * 16;
  u32 kb = (u32)kvoff * 2u, so0 = (wave >> 2) * 4096, keep0, rkn = W * DH * 2, rkf = 0x00020000;
  PIN(vo); PIN(vo_dma);
  asm volatile("s_mov_b32 %0, m0\n\ts_add_u32 m0, %5, %2" : "=&s"(keep0), "+s"(kb), "+s"(so0), "+s"(rkn), "+s"(rkf) : "s"(l0) : "scc");
  SB();
  const bool pre_ = wave < 4 && (xcd & 1) && ((0xD0 >> b) & 1);
  u32x4 cf[2], xr, xr2; u32 xj;
#define GLD(T, p, o) (*reinterpret_cast<const GAS T*>((const GAS unsigned char*)(p) + (o)))
  u32 pf1 = 0;
  if (wave < 4 && (xcd & 1) && (b == 5 || (wave == 0 && b < 4))) {
    const int un = hg * 4 + (b == 5 ? 16 + wave : b);
    u32 op_ = ((16 * un + (lane >> 2)) * NMOD + 128 * (xcd >> 1) + 64 * (lane & 1) + 512 * ((lane >> 1) & 1)) * 2;
    PIN(op_); SB();
    pf1 = GLD(u32, a.wmod, op_);
    SB();
  }
  const u64 kp = (u64)(uintptr_t)a.kc + kb;
  u32x4 rk4 = {(u32)kp, (u32)(kp >> 32) & 0xffffu, rkn, rkf};
  {
    asm volatile("buffer_load_dwordx4 %2, %3, %1 offen nt lds\n\t"
                 "s_add_u32 m0, m0, 0x2000\n\t" "s_add_u32 %1, %1, 0x2000\n\t"
                 "buffer_load_dwordx4 %2, %3, %1 offen nt lds\n\t"
                 "s_add_u32 m0, m0, 0x2000\n\t" "s_add_u32 %1, %1, 0x2000\n\t"
                 "buffer_load_dwordx4 %2, %3, %1 offen nt lds\n\t"
                 "s_add_u32 m0, m0, 0x2000\n\t" "s_add_u32 %1, %1, 0x2000\n\t"
                 "buffer_load_dwordx4 %2, %3, %1 offen nt lds\n\t"
                 "s_mov_b32 m0, %0"
                 : "+s"(keep0), "+s"(so0) : "v"(vo_dma), "s"(rk4) : "memory", "scc");
    SB();
  }
  const __amdgpu_buffer_rsrc_t rk = MKRS(kp, W * DH * 2);
  const u32 t0 = (u32)__builtin_amdgcn_s_memrealtime();
  SB();
  asm volatile("" :: "s"(rk4));
  if (pre_) {
    u32 oc_ = ((n & 7) * D + 128 * wave + 32 * (n >> 3) + 8 * g) * 2, ox_ = (2 * wave * D + lane * 16) * 2;
    u32 oj_ = ((lane & 7) * D + ((32 * (4 * (xcd >> 1) + hg) + 8 * ((lane >> 3) & 3) + 2 * wave) & (D - 1))) * 2;
    PIN(oc_); PIN(ox_); PIN(oj_); SB();
    cf[0] = GLD(u32x4, a.c, oc_); cf[1] = GLD(u32x4, a.c + 64, oc_);
    xr = GLD(u32x4, a.x, ox_); xr2 = GLD(u32x4, a.x + 8, ox_); xj = GLD(u32, a.x, oj_);
    SB();
  }
  const int xe = xcd >> 1;
  const bool odd = xcd & 1;
  const int ncg = odd && xcd != 7 ? 6 : 5;
  const int ui = 3 * hg + b;
  const bool is_unit = b < 3 && ui < 2 * ncg;
  const int cg0 = 5 * xcd + xe, cg = cg0 + (ui >> 1), ub = 4 * (ui & 1);
  const bool is_red = b >= 6;
  const int rb = 2 * hg + (b & 1), rc = xcd + 8 * (wave & 3);
  const bool is_urg = odd && b == 7;
  const bool is_prod = !odd && b >= 4;
  const bool is_mod = odd && (b == 4 || b == 6);
  const bool has_tile = is_urg || is_prod || is_mod;
  const __amdgpu_buffer_rsrc_t rw = MKRS(a.ws, WS_SPAN);
  unsigned char* const tw = (unsigned char*)(((uintptr_t)a.ws + WS_TAIL + (2 << 20) - 1) & ~(uintptr_t)((2 << 20) - 1));
  const __amdgpu_buffer_rsrc_t rt = MKRS(tw, 2 << 20);
  u32* s_fl = reinterpret_cast<u32*>(lds + L_FL);
  float* s_ml = reinterpret_cast<float*>(lds + L_ML);
  float* s_pt = reinterpret_cast<float*>(lds + L_PT);
  if (t < 32 + 32 + 256) s_fl[t] = 0u;

  if (wave >= 4) {
    const int sw = wave - 4;
    const __amdgpu_buffer_rsrc_t rv = MKRS(a.vc + kvoff, W * DH * 2);
    BARRIER();
    {
      u32 keep, so = 8 * 4096;
#define DMA_LOOP(END) asm volatile("s_nop 4\n\t" \
                   "s_mov_b32 %0, m0\n\t" \
                   "s_add_u32 m0, %4, %1\n\t" \
                   "s_nop 0\n" \
                   "1:\n\t" \
                   "buffer_load_dwordx4 %2, %3, %1 offen nt lds\n\t" \
                   "s_add_u32 %1, %1, 0x1000\n\t" \
                   "s_add_u32 m0, m0, 0x1000\n\t" \
                   "s_waitcnt vmcnt(%5)\n\t" \
                   "s_cmp_lt_u32 %1, %6\n\t" \
                   "s_cbranch_scc1 1b\n\t" \
                   "s_mov_b32 m0, %0" \
                   : "=&s"(keep), "+s"(so) : "v"(vo_dma), "s"(rk), "s"(l0), "i"(PFS - 1), "s"((END) * 4096) : "memory", "scc")
      if (has_tile) { DMA_LOOP(is_mod ? NPAUSEM : is_prod ? NPAUSEQ : NPAUSE); BARRIER(); BARRIER(); }
      DMA_LOOP(32);
    }
    SB();
    if (sw == 0) STAMP(28);
    u32x4 kr[32], vr[64];
    u32 a_pg = LADDR(L_PG + sw * 256 + lane * 4);
    PIN(a_pg);
    u64 tn, re;
    u32x16 rs;
    u32 tg = 0;
    u32 tref = t0 - (1u << 30);
#define CLK() asm volatile("s_memrealtime %0" : "=s"(tn))
    int dlag = 1 << 29;
#define SAMPLE(i) do { if (((i) % CEV) == 0) { asm volatile("s_waitcnt lgkmcnt(0)" : "+s"(tn)); dlag = (int)((u32)tn - tref); \
      if (CCODE) asm volatile("s_memrealtime %0" : "=s"(tn) : "s"(dlag)); } } while (0)
#define SREF(i) sched(((i) / CEV) * CEV - 1)
#define CTRL(i, HI, LO) do { if (!CCODE) { VMWAIT((HI) - 1); break; } SAMPLE(i); \
      asm volatile("s_cmp_ge_i32 %0, %3\n\ts_cbranch_scc1 1f\n\ts_waitcnt vmcnt(%2)\n1:\n\ts_waitcnt vmcnt(%1)" :: "s"(dlag), "i"((HI) - 1), "i"((LO) - 1), "i"(SREF(i)) : "memory", "scc"); } while (0)
#define CTRLP(i, LO) do { if (!CCODE) break; SAMPLE(i); \
      asm volatile("s_cmp_ge_i32 %0, %2\n\ts_cbranch_scc1 1f\n\ts_waitcnt vmcnt(%1)\n1:" :: "s"(dlag), "i"((LO) - 1), "i"(SREF(i)) : "memory", "scc"); } while (0)
    if (CT && RDREF) asm volatile("s_load_dwordx16 %0, %2, %3\n\ts_load_dwordx2 %1, %2, %4" : "=&s"(rs), "=&s"(re) : "s"(a.ws), "i"(WS_RF), "i"(WS_EPOCH) : "memory");
#define VISSUE(i) do { vr[i] = BLD(rv, vo, (i) * 4096, 2); if ((i) + 1 >= PK0) { SB(); *LPTR(volatile u32, a_pg) = (i) + 1; } } while (0)
#pragma unroll
    for (int i = 0; i < 32; ++i) { kr[i] = BLD(rk, vo, 131072 + i * 4096, 2); SB();
      if (CT && !RDREF && i == RI) { CLK(); SB(); }
      if (CT && RDREF && i == RI) {
        asm volatile("s_waitcnt lgkmcnt(0)" : "+s"(rs), "+s"(re));
        tg = (u32)re + 1u;
        int sum = 0, cnt = 0;
#pragma unroll
        for (int k = 0; k < 8; ++k) { const bool v_ = rs[2 * k + 1] == tg; sum += v_ ? (int)(rs[2 * k] - t0) : 0; cnt += v_; }
        const u32 tr_ = t0 + (u32)(int)((float)sum * __builtin_amdgcn_rcpf((float)cnt));
        tref = __builtin_amdgcn_readfirstlane(cnt ? tr_ : tref);
        if (HG && cnt && sw == 0 && lane == 0) __hip_atomic_store(s_fl + 8, tref | 1u, RLX_WG);
        tref += is_unit ? -UOFS : NOFS;
        REF_STAMP();
        CLK();
        SB();
      }
      if (CT && i > RI) CTRL(32 + i, PFS, PLK); else VMWAIT(PFS - 1);
      if (i == PFS - 1) LDS_ARRIVE(s_fl + 5); SB(); }
    if (sw == 0) STAMP(29);
#pragma unroll
    for (int i = 0; i < NPRE; ++i) {
      VISSUE(i); SB();
      if (i + 1 < NPRE) { if (CT && CVP) CTRL(64 + i, PFS, PLV); else VMWAIT(PFS - 1); }
      SB();
      if (CT && A2 && 64 + i == AI + CEV && jj == JS && sw == 0) __builtin_amdgcn_raw_buffer_store_b64(u32x2{(u32)dlag + tref, tg}, rw, lane == 0 ? WS_RF + 128 + xcd * 8 : OOB, 0, 16);
    }
    if (CT) asm volatile("s_waitcnt lgkmcnt(0)" : "+s"(tn));
    if (sw == 0) STAMP(16);
    LDS_SPIN(s_fl + 6, 1u);
    if (sw == 0 && w != EPW) STAMP(17);
    const u32x4 qp = *reinterpret_cast<const u32x4*>(lds + L_Q + n * 16);
    float sv[2];
#pragma unroll
    for (int G = 0; G < 2; ++G) {
      float xx[16];
#pragma unroll
      for (int c = 0; c < 16; ++c) {
        const int i = 16 * G + c;
        SB();
        float d = dot2(kr[i][0], qp[0], 0.f); d = dot2(kr[i][1], qp[1], d); d = dot2(kr[i][2], qp[2], d); d = dot2(kr[i][3], qp[3], d);
        xx[c] = d; PIN(xx[c]);
        SB();
        if ((i + 1) * NQ / 32 > i * NQ / 32) VISSUE(NPRE + i * NQ / 32);
        if (CT && A2 && i == AR) asm volatile("s_load_dwordx16 %0, %1, %2" : "=&s"(rs) : "s"(a.ws), "i"(WS_RF + 128) : "memory");
      }
      SB();
      sv[G] = rbf(rbf(row_tr_reduce16(xx, lane)) * SCALE);
    }
    s_pt[(sw * 4 + g) * 68 + 32 + n] = sv[0];
    s_pt[(sw * 4 + g) * 68 + 48 + n] = sv[1];
    {
      const float mw = wave_allmax(fmaxf(sv[0], sv[1]));
      const float lw = wave_allsum(__expf(sv[0] - mw) + __expf(sv[1] - mw));
      if (lane == 0) *reinterpret_cast<f32x2*>(s_ml + wave * 2) = f32x2{mw, lw};
    }
    LDS_ARRIVE(s_fl + 7);
    SB();
#pragma unroll
    for (int i = N1; i < N2 - 1; ++i) { VISSUE(i); SB(); }
    if (CT && A2) {
      asm volatile("s_waitcnt lgkmcnt(0)" : "+s"(rs));
      int sum = 0, cnt = 0;
#pragma unroll
      for (int k = 0; k < 8; ++k) { const bool v_ = rs[2 * k + 1] == tg; sum += v_ ? (int)(rs[2 * k] - t0) : 0; cnt += v_; }
      const u32 tr_ = t0 + (u32)(int)((float)sum * __builtin_amdgcn_rcpf((float)cnt)) - (u32)(sched(AI) - PSH) + (is_unit ? -UOFP : NOFP);
      tref = __builtin_amdgcn_readfirstlane(cnt >= AMIN ? tr_ : tref);
      REF_STAMP2();
      SB();
    }
    if (sw == 0 && w != EPW) STAMP(18);
    LDS_SPIN(s_fl + 7, 8u);
    {
      const f32x4 m0 = *reinterpret_cast<const f32x4*>(s_ml), m1 = *reinterpret_cast<const f32x4*>(s_ml + 4);
      const f32x4 m2 = *reinterpret_cast<const f32x4*>(s_ml + 8), m3 = *reinterpret_cast<const f32x4*>(s_ml + 12);
      f32x4* pp = reinterpret_cast<f32x4*>(s_pt + (sw * 4 + g) * 68 + 4 * n);
      const f32x4 s4 = *pp;
      const float m = fmaxf(fmaxf(fmaxf(m0[0], m0[2]), fmaxf(m1[0], m1[2])), fmaxf(fmaxf(m2[0], m2[2]), fmaxf(m3[0], m3[2])));
      const float l = ((m0[1] * __expf(m0[0] - m) + m0[3] * __expf(m0[2] - m)) + (m1[1] * __expf(m1[0] - m) + m1[3] * __expf(m1[2] - m))) +
                      ((m2[1] * __expf(m2[0] - m) + m2[3] * __expf(m2[2] - m)) + (m3[1] * __expf(m3[0] - m) + m3[3] * __expf(m3[2] - m)));
      const float il = 1.f / l;
      u32x4 p4;
#pragma unroll
      for (int i = 0; i < 4; ++i) { const float p = __expf(s4[i] - m) * il; p4[i] = cvtpk(p, p); }
      *reinterpret_cast<u32x4*>(pp) = p4;
      SB();
      VISSUE(N2 - 1);
      SB();
      if (CT && CPV) { CLK(); dlag = 1 << 29; }
      LDSWAIT();
    }
    if (sw == 0 && (w & 63) != 47) STAMP(19);
    const __amdgpu_buffer_rsrc_t rg = MKRS(a.wg + cg * 32, is_unit ? D * F * 2 - cg * 64 : 0), ru = MKRS(a.wu + cg * 32, is_unit ? D * F * 2 - cg * 64 : 0);
    int vo_g = (128 * sw + 8 * g + (n >> 2)) * (F * 2) + ((lane & 3) ^ (lane >> 5)) * 16, vo_u = vo_g ^ 32;
    u32 a_op = LADDR(L_OP + sw * OROW + (n >> 1) * 32 + (2 * (n & 1) + (g & 1)) * 4 + (g >> 1) * 2048);
    u32 a_rr = LADDR(L_K + ((lane >> 1) & 1) * 32768 + (8 * g + (n >> 2)) * 64 + (sw ^ ((g & 1) + (lane & 2))) * 16 + (lane & 1) * 8);
    PIN(a_op); PIN(a_rr); PIN(vo_g); PIN(vo_u);
    asm volatile("" :: "s"(rg), "s"(ru));
    u32 msk[4];
#pragma unroll
    for (int j = 0; j < 4; ++j) msk[j] = ((n >> 1) == j) ? ((n & 1) ? 0xffff0000u : 0x0000ffffu) : 0u;
    f32x4 oacc = {0.f, 0.f, 0.f, 0.f};
    const unsigned char* prow = lds + L_PT + (sw * 4 + g) * 68 * 4;
    u32x4 pq[2];
    pq[0] = *reinterpret_cast<const u32x4*>(prow);
#define PVSTAMP(i) do {} while (0)
#define PV1(I) { constexpr int i = (I); \
      SB(); \
      if ((i & 3) == 0 && i + 4 < 64) pq[((i >> 2) + 1) & 1] = *reinterpret_cast<const u32x4*>(prow + (i + 4) * 4); \
      const u32 pd = pq[(i >> 2) & 1][i & 3]; \
      const u32x4 af = {pd & msk[0], pd & msk[1], pd & msk[2], pd & msk[3]}; \
      oacc = mfma(af, vr[i], oacc); \
      SB(); \
      static_assert(issued(i) - issued(i - 1) <= 1, ""); \
      if (issued(i) > issued(i - 1)) { constexpr int j = issued(i - 1) < 64 ? issued(i - 1) : 63; if (CT && CPV) { CTRLP(64 + j, PLP); SB(); } VISSUE(j); SB(); } \
      PVSTAMP(i); \
      if (i == TAILPF) { LDS_ARRIVE(s_fl + 13); SB(); } }
#define PV4(I) PV1(I) PV1((I) + 1) PV1((I) + 2) PV1((I) + 3)
#define PV16(I) PV4(I) PV4((I) + 4) PV4((I) + 8) PV4((I) + 12)
    PV16(0) PV16(16) PV16(32) PV16(48)
    if (CT) asm volatile("s_waitcnt lgkmcnt(0)" : "+s"(tn));
    SB();
    *LPTR(u32, a_op) = cvtpk(oacc[0], oacc[1]); *LPTR(u32, a_op + 16) = cvtpk(oacc[2], oacc[3]);
    if (sw == 0) STAMP(20);
    BARRIER();
    if (!is_unit) EXIT();
    {
      u32 keep;
      asm volatile("s_mov_b32 %0, m0" : "=s"(keep));
#pragma unroll
      for (int e = 0; e < 16; ++e) {
        if (e == B2B) { __builtin_amdgcn_s_barrier(); SB(); }
        const int it = (e & 3) + 4 * (e >> 3), mt = (e >> 2) & 1;
        asm volatile("s_add_u32 m0, %2, %3\n\ts_nop 0\n\tbuffer_load_dwordx4 %0, %1, %4 offen lds"
                     :: "v"(mt ? vo_u : vo_g), "s"(mt ? ru : rg), "s"(LADDR(L_K) + sw * 8192), "i"(mt * 32768 + it * 1024), "s"((32 * (it >> 1) + 4 * (it & 1)) * (F * 2)) : "memory", "scc");
        SB();
      }
      asm volatile("s_mov_b32 m0, %0" :: "s"(keep));
    }
    __builtin_amdgcn_s_barrier();
    SB();
    if (sw == 0) STAMP(1);
    u32x8 gf[8];
#define GF_READ(s) do { _Pragma("unroll") for (int u = 0; u < 4; ++u) { \
      const u32x2 t_ = __builtin_bit_cast(u32x2, __builtin_amdgcn_ds_read_tr16_b64_v4i16(LPTR(s16x4, a_rr + (s) * 4096 + (u >> 1) * 2048 + (u & 1) * 256))); \
      gf[s][2 * u] = t_[0]; gf[s][2 * u + 1] = t_[1]; } } while (0)
#define LDS_SPIN0(ptr, n) do { for (unsigned spin_ = 0; __hip_atomic_load((ptr), RLX_WG) < (n) && spin_ < (SPIN_CAP << 3); ++spin_) {} MEMFENCE(); } while (0)
    int sidx = (n & 2) ? 0xEEEE : 0x4444;
    f32x4 acc = {0.f, 0.f, 0.f, 0.f}, acs = {0.f, 0.f, 0.f, 0.f}, acd = {0.f, 0.f, 0.f, 0.f};
    PIN(sidx); PIN(acc); PIN(acs); PIN(acd);
    u32 a_h2 = LADDR(L_H2 + (n >> 2) * HROW + g * 32 + (n & 2) * 8);
    u32 a_hw = LADDR(L_HH + (n >> 3) * 256 + g * 64 + (sw >> 1) * 32 + ((n >> 1) & 1) * 16 + (2 * (sw & 1) + ((n >> 2) & 1)) * 4 + (n & 1) * 2);
    u32 a_st = LADDR(L_PG + g * 256 + g * 16), a_z = a_h2 + (n & 1) * 128;
    PIN(a_h2); PIN(a_hw); PIN(a_st); PIN(a_z);
    constexpr float L2E = 1.4426950408889634f;
    float kk = n < 8 ? L2E : 1.f, mz = *LPTR(const float, LADDR(L_H2 + 1024) + g * HROW);
    float kd0 = n < 8 ? 1.f / (D * L2E) : 1.f / D, kd1 = n < 8 ? 1.f / (D * L2E * L2E) : 1.f / D, ke = n < 8 ? 1e-5f / (L2E * L2E) : 1e-5f;
    PIN(kk); PIN(kd0); PIN(kd1); PIN(ke);
    u32x4 af[8];
#pragma unroll
    for (int s = 0; s < 8; ++s) af[s] = *LPTR(const u32x4, a_h2 + (4 + (n >> 2) + (n & 1)) * HROW + s * 128);
    SB();
    PIN(mz);
    VMWAIT(8);
    LDS_ARRIVE(s_fl + 4);
    LDS_SPIN0(s_fl + 4, 4u);
#pragma unroll
    for (int s = 0; s < 8; s += 2) GF_READ(s);
    SB();
#pragma unroll
    for (int s = 0; s < 8; s += 2) acs = smfmac(af[s], gf[s], acs, sidx);
    SB();
    VMWAIT(0);
    if (sw == 0) STAMP(31);
    LDS_ARRIVE(s_fl + 3);
    LDS_SPIN0(s_fl + 3, 4u);
#pragma unroll
    for (int s = 1; s < 8; s += 2) GF_READ(s);
    SB();
#pragma unroll
    for (int s = 1; s < 8; s += 2) acs = smfmac(af[s], gf[s], acs, sidx);
    PIN(acs);
    SB();
    if (sw == 0) STAMP(24);
    BARRIER();
    if (sw == 0) STAMP(25);
    {
      u32x4 az[4];
#pragma unroll
      for (int s = 0; s < 4; ++s) az[s] = *LPTR(const u32x4, a_z + s * 256);
      SB();
      f32x2 cs = fold(acs);
      cs[1] *= kk;
      PIN(cs);
      SB();
      f32x4 st;
      float s1, s2, rs, dm;
#pragma unroll
      for (int s = 0; s < 8; ++s) {
        const int k = s == 5 ? 6 : s == 6 ? 5 : s;
        if (k & 1) acd = smfmac(az[k >> 1], gf[k], acd, sidx); else acc = smfmac(az[k >> 1], gf[k], acc, sidx);
        SB();
        if (s == 0) {
          asm volatile("s_barrier" ::: "memory");
          SB();
          st = *LPTR(const f32x4, a_st);
        }
        if (s == 3) { s1 = st[0] + st[2]; PIN(s1); s2 = st[1] + st[3]; PIN(s2); }
        if (s == 4) { rs = s1 * kd0; PIN(rs); s2 = fmaf(s2, kd1, ke); PIN(s2); }
        if (s == 5) { rs = fmaf(-rs, rs, s2); asm volatile("v_max_f32 %0, %0, %1" : "+v"(rs) : "v"(ke)); }
        if (s == 6) { rs = __builtin_amdgcn_rsqf(rs); PIN(rs); }
        if (s == 7) { dm = fmaf(s1, -1.f / D, mz) * cs[0]; PIN(dm); dm = fmaf(rs, dm, cs[1]); PIN(dm); }
        SB();
      }
      float u_ = fmaf(rs, acc[0] + acc[2], dm);
      PIN(u_);
      SB();
      const float gv = fmaf(rs, acd[1] + acd[3], u_);
      const float hv = dppf<XOR8>(gv) * __builtin_amdgcn_rcpf(1.f + __builtin_amdgcn_exp2f(-gv));
      *LPTR(u16, a_hw) = (u16)cvtpk(hv, hv);
    }
    BARRIER();
    asm volatile(".Lkb_pf_end:");
    EXIT();
  }

  const int cw = wave;
  const bool prb = (w & 63) == (EPW & 63);
  if (prb) {
#pragma unroll
    for (int i = 0; i < NPROBE; ++i) __builtin_amdgcn_raw_buffer_store_b128(u32x4{0u, 0u, 0u, 0u}, rt, T_PR + (hg * 4 + cw) * 128 + (lane >> 3) * 4096 + (lane & 7) * 16, i * 2048, 16);
    SB();
  }
  const u64 E = __hip_atomic_load((GAS u64*)(a.ws + WS_EPOCH), RLX_AGENT);
  u32 pf0 = 0, pf2 = 0;
  {
    u64 ps = (u64)(uintptr_t)(odd ? a.wmod + 1024 + 512 * xe : a.wq + 1024 * xe);
    u32 po = (8 * ((hg * 4 + b) * 4 + cw) + (lane >> 3)) * (odd ? NMOD * 2 : H * DH * 2) + 128 * (lane & 7);
    asm volatile("" : "+s"(ps));
    PIN(po); PIN(pf0); PIN(pf2);
    SB();
    if (b < 4) {
      pf0 = GLD(u32, ps, po);
      if (!odd) pf2 = GLD(u32, ps + 1024, po);
    }
  }
  SB();
  u32 th;
#define GET_TAG() do { th = __builtin_amdgcn_readfirstlane((u32)(E + 1)); } while (0)
  u32x4 wr[32];
  const int bq = lane & 7, ch8 = n & 7;
  const int trow = 128 * cw + 32 * (n >> 3) + 8 * g;
  float* s_mp = reinterpret_cast<float*>(lds + L_MP);
#define TILE_ISSUE(RS, VO, STRIDE) do { BARRIER(); _Pragma("unroll") for (int i = 0; i < 16; ++i) { wr[i] = BLD(RS, VO, (64 * (i >> 3) + (i & 7)) * (STRIDE), 0); SB(); } BARRIER(); } while (0)
#define TILE_MFMA(A0, A1) do { f32x4 acc_[8]; \
    _Pragma("unroll") for (int c = 0; c < 8; ++c) acc_[c] = mfma(A0, wr[c], f32x4{0.f, 0.f, 0.f, 0.f}); \
    _Pragma("unroll") for (int c = 0; c < 8; ++c) acc_[c] = mfma(A1, wr[8 + c], acc_[c]); \
    if ((g >> 1) == (n >> 3)) { _Pragma("unroll") for (int c = 0; c < 8; ++c) *reinterpret_cast<f32x4*>(s_mp + (cw * 2 + (n >> 3)) * 512 + (c * 8 + ch8) * 8 + 4 * (g & 1)) = acc_[c]; } \
    LDS_ARRIVE(s_fl); LDS_SPIN(s_fl, 4u); } while (0)
#define TILE_SUM2(C, CH, PAIR) do { f32x2 v_ = {0.f, 0.f}; \
    _Pragma("unroll") for (int p = 0; p < 8; ++p) v_ += *reinterpret_cast<const f32x2*>(s_mp + p * 512 + ((C) * 8 + (CH)) * 8 + 2 * cw); \
    auto s_ = __builtin_amdgcn_permlane32_swap(__float_as_uint(v_[0]), __float_as_uint(v_[1]), false, false); \
    PAIR = cvtpk(__uint_as_float(s_[0]), __uint_as_float(s_[1])); } while (0)
  const __amdgpu_buffer_rsrc_t ro = MKRS(a.wo + (size_t)h * DH * D, DH * D * 2);
  const int vo_o = g * 8 * (D * 2) + cw * 256 + n * 16;
#define WO_ISSUE(j) wr[((j) + 16) & 31] = BLD(ro, vo_o, (32 * ((((j) + 16) & 31) >> 3) + ((j) & 7)) * (D * 2), 0)
  if (is_urg || is_mod) {
    const int mi = is_urg ? 4 * xe + hg : 8 * xe + 2 * hg + ((b >> 1) & 1);
    const __amdgpu_buffer_rsrc_t rm = MKRS(a.wmod, D * NMOD * 2);
    const int vo_m = trow * (NMOD * 2) + (is_urg ? 32 * mi + 8 * (ch8 & 3) + 512 * (ch8 >> 2) : 1024 + 64 * mi + 8 * ch8) * 2;
    const int ch = (lane >> 3) & 3, cc = 2 * cw + (lane >> 5);
    SB();
    BARRIER();
#pragma unroll
    for (int gr = 0; gr < 2; ++gr)
#pragma unroll
      for (int j = 0; j < 4; ++j) {
        const float a0 = lo2f(cf[gr][j]), a1 = hi2f(cf[gr][j]);
        cf[gr][j] = cvtpk(a0 * __builtin_amdgcn_rcpf(1.f + __expf(-a0)), a1 * __builtin_amdgcn_rcpf(1.f + __expf(-a1)));
      }
    PIN(cf[0]); PIN(cf[1]);
    {
      float xf[16];
#pragma unroll
      for (int j = 0; j < 4; ++j) { xf[2 * j] = lo2f(xr[j]); xf[2 * j + 1] = hi2f(xr[j]); xf[8 + 2 * j] = lo2f(xr2[j]); xf[9 + 2 * j] = hi2f(xr2[j]); }
      float s1 = 0.f;
#pragma unroll
      for (int j = 0; j < 16; ++j) s1 += xf[j];
      const float mean = half_allsum(s1) * (1.f / D);
      float s2 = 0.f;
#pragma unroll
      for (int j = 0; j < 16; ++j) { const float d = xf[j] - mean; s2 = fmaf(d, d, s2); }
      const float var = half_allsum(s2) * (1.f / D);
      if ((lane & 31) == 0) *reinterpret_cast<f32x2*>(s_ml + (2 * cw + (lane >> 5)) * 2) = f32x2{rbf(mean), rbf(sqrtf(rbf(rbf(var) + 1e-5f)))};
    }
    GET_TAG();
    SB();
    TILE_ISSUE(rm, vo_m, NMOD * 2);
    to_frags(wr); to_frags(wr + 8);
    TILE_MFMA(cf[0], cf[1]);
    if (is_urg) {
      const int oa = (cc * 8 + ch) * 8 + bq;
      float va = 0.f, vb = 0.f;
#pragma unroll
      for (int p = 0; p < 8; ++p) { va += s_mp[p * 512 + oa]; vb += s_mp[p * 512 + oa + 32]; }
      const f32x2 st = *reinterpret_cast<const f32x2*>(s_ml + bq * 2);
      PIN(xj);
      const float ln = rbf(rbf(((lane >> 5) ? hi2f(xj) : lo2f(xj)) - st[0]) / st[1]);
      const float hv = rbf(rbf(ln * rbf(1.f + rbf(vb))) + rbf(va));
      auto sw2 = __builtin_amdgcn_permlane32_swap(__float_as_uint(hv), __float_as_uint(hv), false, false);
      if (lane < 32) PUBLISH((sw2[0] >> 16) | (sw2[1] & 0xffff0000u), rw,
                             WS_HG + (((mi >> 1) * 2 + (cw >> 1)) * 64 + ch * 16 + (mi & 1) * 8 + bq) * 16 + (cw & 1) * 8, 0);
      CSTAMP(3);
    } else {
      u32 pr;
      TILE_SUM2(4 * ((lane >> 3) & 1) + 2 * (lane & 1) + (lane >> 5), 4 * (g & 1) + ((lane >> 1) & 3), pr);
      PUBLISH(pr, rw, mg_off(2 * cw + (lane >> 5), mi >> 3) + ((mi & 7) * 32 + (lane & 31)) * 8, 0);
      CSTAMP(7);
    }
  } else if (is_prod) {
    const int qi = 16 * xe + 4 * hg + (b - 4);
    const __amdgpu_buffer_rsrc_t rq = MKRS(a.wq, D * H * DH * 2);
    const int vo_q = trow * (H * DH * 2) + (64 * qi + 8 * ch8) * 2;
    BARRIER();
    TILE_ISSUE(rq, vo_q, H * DH * 2);
    GET_TAG();
    to_frags(wr); to_frags(wr + 8);
    SB();
#pragma unroll
    for (int j = 0; j < NQA; ++j) { WO_ISSUE(j); SB(); VMWAIT(PWA - 1); SB(); }
    WAIT_T(THP);
    const int vo_h = WS_HG + cw * 4096 + lane * 16;
    u32x4 hq[4];
    POLL_BEGIN()
#pragma unroll
      for (int i = 0; i < 4; ++i) hq[i] = BLD(rw, vo_h, i * 1024, 16);
      const bool ok = ok2(hq[0], th) & ok2(hq[1], th) & ok2(hq[2], th) & ok2(hq[3], th);
    POLL_END(ok)
    CSTAMP(4);
    const u32x4 a0 = {hq[0][0], hq[0][2], hq[1][0], hq[1][2]}, a1 = {hq[2][0], hq[2][2], hq[3][0], hq[3][2]};
    TILE_MFMA(a0, a1);
    u32 pr;
    TILE_SUM2(2 * (lane & 3) + (lane >> 5), (lane >> 2) & 7, pr);
    PUBLISH(pr, rw, q_off(2 * cw + (lane >> 5), qi >> 1) + (32 * (qi & 1) + (lane & 31)) * 8, 0);
    CSTAMP(5);
  } else {
    BARRIER();
    if (CT && !NOPUB && jj == JS && cw == 3) {
      u64 e_, c_;
      asm volatile("s_load_dwordx2 %0, %2, 0\n\ts_waitcnt vmcnt(%3)\n\ts_memrealtime %1\n\ts_waitcnt lgkmcnt(0)" : "=&s"(e_), "=&s"(c_) : "s"(a.ws + WS_EPOCH), "i"(SWT) : "memory");
      __builtin_amdgcn_raw_buffer_store_b64(u32x2{t0 + (((u32)c_ - t0) * RAL >> 4), (u32)(e_ + 1)}, rw, lane == 0 ? WS_RF + xcd * 8 : OOB, 0, 16);
    }
  }
  SB();
  asm volatile("" :: "v"(pf0), "v"(pf1), "v"(pfc), "v"(pf2));
  GET_TAG();
  LDS_ARRIVE(s_fl + 5);
  if (prb) {
    const unsigned char* pp = tw + T_PR + (hg * 4 + cw) * 128;
#pragma nounroll
    for (int p = 0; p < 8 * NPROBE; ++p) {
      const int k = (p + 2 * cw + hg) & 7;
      u64 c0, c1; u32 z;
      asm volatile("s_memtime %0\n\ts_load_dword %2, %3, %4\n\ts_waitcnt lgkmcnt(0)\n\ts_memtime %1\n\ts_waitcnt lgkmcnt(0)"
                   : "=&s"(c0), "=&s"(c1), "=&s"(z) : "s"(pp), "s"(k * 4096 + (p >> 3) * 2048) : "memory");
      if (lane == 0) __hip_atomic_fetch_max(s_fl + 16 + k, 4095u - min((u32)c1 - (u32)c0, 4095u), RLX_WG);
    }
    LDS_ARRIVE(s_fl + 10);
    if (cw == 3) {
      LDS_SPIN(s_fl + 10, 4u);
      const u32x4 ma = *reinterpret_cast<const u32x4*>(s_fl + 16), mb = *reinterpret_cast<const u32x4*>(s_fl + 20), mc = ma + mb, md = mb - ma;
#define VOTE(x) (u32)min(max(((int)(x) >> 3) + 128, 0), 255)
      PUBLISH(VOTE(mc[1] + mc[2] - mc[0] - mc[3]) | VOTE(mc[2] + mc[3] - mc[0] - mc[1]) << 10 | VOTE(md[0] + md[1] + md[2] + md[3]) << 20, rt, lane < 8 ? T_CP + lane * 128 + hg * 32 : OOB, 0);
      STAMP(19);
    }
  }
  const int nwa = prb ? (cw ? NWE : NWM0) : is_prod ? (cw ? NWQ : NQA) : has_tile ? (cw ? NWM : NWM0) : (cw ? NWP : NWP0);
#pragma unroll
  for (int j = 0; j < NWP; ++j) {
    if (j < nwa && (j >= NQA || !is_prod)) { WO_ISSUE(j); SB(); VMWAIT(PWA - 1); SB(); }
  }
  if (cw == 0) {
    CSTAMP2(22);
    WAIT_T(TQP);
    u32x2 qg;
    POLL_BEGIN()
      qg = BLD64(rw, q_off(bd, h) + lane * 8, 0, 16);
      const bool ok = qg[1] == th;
      __builtin_amdgcn_s_sleep(4);
    POLL_END(ok)
    *reinterpret_cast<u32*>(lds + L_Q + lane * 4) = qg[0];
    STAMP(6);
    LDS_ARRIVE(s_fl + 6);
  }
  if (PADC) asm volatile("s_branch 1f\n\t.fill %0, 4, 0xBF800000\n1:" :: "i"(PADC / 4 - 1));
  LDS_SPIN(s_fl + 6, 1u);
  LDS_SPIN(s_fl + 5, 8u);
  {
    u32x4 qa[4];
#pragma unroll
    for (int j = 0; j < 4; ++j) qa[j] = *reinterpret_cast<const u32x4*>(lds + L_Q + j * 64 + g * 16);
    f32x4 acc[8];
#pragma unroll
    for (int T = 0; T < 8; ++T) {
      acc[T] = f32x4{0.f, 0.f, 0.f, 0.f};
#pragma unroll
      for (int j = 0; j < 4; ++j) {
        const u32x4 kf = *reinterpret_cast<const u32x4*>(lds + L_K + (128 * cw + n) * 256 + (((4 * j + g) ^ n) << 4) + T * 4096);
        acc[T] = mfma(qa[j], kf, acc[T]);
      }
    }
    const float s0 = rbf(rbf(sel4(acc[0][0], acc[1][0], acc[2][0], acc[3][0], g)) * SCALE);
    const float s1 = rbf(rbf(sel4(acc[4][0], acc[5][0], acc[6][0], acc[7][0], g)) * SCALE);
    s_pt[n * 68 + 8 * cw + g] = s0;
    s_pt[n * 68 + 8 * cw + 4 + g] = s1;
    const float mw = wave_allmax(fmaxf(s0, s1));
    const float lw = wave_allsum(__expf(s0 - mw) + __expf(s1 - mw));
    if (lane == 0) *reinterpret_cast<f32x2*>(s_ml + wave * 2) = f32x2{mw, lw};
  }
  LDS_ARRIVE(s_fl + 7);
  if (cw == 3) {
    u32x2 va;
    POLL_BEGIN()
      va = BLD64(rt, T_CP + xcd * 128 + (lane & 3) * 32, 0, 16);
      const bool ok = va[1] == th;
      __builtin_amdgcn_s_sleep(4);
    POLL_END(ok)
    u32 vs = va[0] + (u32)__builtin_amdgcn_mov_dpp((int)va[0], XOR1, 0xf, 0xf, true);
    vs += (u32)__builtin_amdgcn_mov_dpp((int)vs, XOR2, 0xf, 0xf, true);
    if (lane == 0) __hip_atomic_store(s_fl + 9, ((((vs << 3) ^ (vs >> 7)) & 4096u) | ((vs >> 6) & 8192u) | ((vs >> 15) & 16384u)) ^ 0x3001u, RLX_WG);
  }
  u32x8 wf[16];
  int cp;
  u32 zs;
  const int lvl = __builtin_amdgcn_readfirstlane(nwa >= 18 ? 0 : nwa >= 10 ? 1 : 2);
  int nwb = __builtin_amdgcn_readfirstlane(nwa);
  asm volatile("" : "+s"(nwb));
  {
#pragma unroll
    for (int j = 0; j <= JD0; ++j) {
      if (j == (PSJ < JD0 ? PSJ : JD0)) {
        LDS_SPIN(s_fl + 9, 1u);
        cp = __builtin_amdgcn_readfirstlane(s_fl[9]) & 0x7000;
        const int c_ = cls(xcd) ^ cp;
        asm volatile("s_load_dword %0, %1, 0\n\ts_load_dword %0, %1, 64\n\ts_load_dword %0, %1, 128\n\ts_load_dword %0, %1, 192\n\t"
                     "s_load_dword %0, %1, 256\n\ts_load_dword %0, %1, 320\n\ts_load_dword %0, %1, 384\n\ts_load_dword %0, %1, 448\n\t"
                     "s_load_dword %0, %2, 0\n\ts_load_dword %0, %2, 64\n\ts_load_dword %0, %2, 128\n\ts_load_dword %0, %3, 0\n\ts_load_dword %0, %3, 64"
                     : "=&s"(zs) : "s"(tw + (T_IN + (jj >> 1) * 32768 + c_ + (jj & 1) * 2048 + cw * 512)),
                       "s"(tw + (T_INX + (jj >> 2) * 32768 + (c_ ^ 4096) + (jj & 3) * 1024 + cw * 192)),
                       "s"(tw + (T_X1 + (jj >> 3) * 32768 + (c_ ^ 16384) + (jj & 7) * 512 + cw * 128)) : "memory");
        SB();
      }
      if (j < JD0 && j >= nwb) {
        WO_ISSUE(j); SB();
        asm volatile("s_waitcnt vmcnt(%3)\n\ts_cmp_gt_i32 %0, 1\n\ts_cbranch_scc1 1f\n\ts_waitcnt vmcnt(%2)\n\ts_cmp_gt_i32 %0, 0\n\ts_cbranch_scc1 1f\n\ts_waitcnt vmcnt(%1)\n1:"
                     :: "s"(lvl), "i"(PB0 - 1), "i"(PB1 - 1), "i"(PB2 - 1) : "memory", "scc");
        SB();
      }
    }
  }
  to_frags2(wr + 16, wf + 8);
#define FRAG2(j) do { _Pragma("unroll") for (int c = 0; c < 8; ++c) { \
    u32 o_ = (c & 1) ? perm_hi(wr[2 * (j)][c >> 1], wr[2 * (j) + 1][c >> 1]) : perm_lo(wr[2 * (j)][c >> 1], wr[2 * (j) + 1][c >> 1]); wf[c][j] = o_; } } while (0)
#pragma unroll
  for (int j = 0; j < (JD0 - 16) / 2; ++j) FRAG2(j);
#pragma unroll
  for (int c = 0; c < 8; ++c) PIN(wf[c]);
  SB();
  asm volatile("s_waitcnt lgkmcnt(0)" : "+s"(zs) :: "memory");
  float e0[8];
  float m0 = 0.f;
  const int rcol = 32 * (rc >> 1) + 4 * (rc & 1) + 8 * (bq >> 1) + 2 * (bq & 1);
  u32 a_h2w = LADDR(L_H2 + cw * HROW + (lane >> 2) * 32 + (lane & 3) * 4);
  if (is_red) {
    const u32 xp = *reinterpret_cast<const u32*>(a.x + rb * D + rcol);
    const int vo_m = mg_off(rb, 0) + (rc * 8 + bq) * 8;
    u32x2 ga, gb;
    POLL_BEGIN()
      ga = BLD64(rw, vo_m, 0, 16); gb = BLD64(rw, vo_m, 3 * 2048, 16);
      const bool ok = (ga[1] == th) & (gb[1] == th);
    POLL_END(ok)
    e0[0] = lo2f(xp); e0[1] = hi2f(xp); e0[2] = lo2f(ga[0]); e0[3] = hi2f(ga[0]); e0[4] = lo2f(gb[0]); e0[5] = hi2f(gb[0]);
#pragma unroll
    for (int i = 0; i < 6; ++i) PIN(e0[i]);
  } else if (is_unit) {
    {
      const int vo_m = mg_off(ub + cw, 0) + lane * 16;
      const u32x4 x0 = *reinterpret_cast<const u32x4*>(a.x + (ub + cw) * D + lane * 8);
      u32x4 m4[4];
      POLL_BEGIN()
#pragma unroll
        for (int i = 0; i < 4; ++i) m4[i] = BLD(rw, vo_m, 2048 + i * 1024, 16);
        const bool ok = ok2(m4[0], th) & ok2(m4[1], th) & ok2(m4[2], th) & ok2(m4[3], th);
      POLL_END(ok)
#pragma unroll
      for (int i = 0; i < 4; ++i) {
        const u32 cbp = m4[2 + (i >> 1)][2 * (i & 1)], c1 = cvtpk(1.f + lo2f(cbp), 1.f + hi2f(cbp));
        *LPTR(u32, a_h2w + (4 + cw) * HROW + (i >> 1) * 512 + (i & 1) * 16) = c1;
        *LPTR(u32, a_h2w + (5 + cw) * HROW + (i >> 1) * 512 + (i & 1) * 16) = m4[i >> 1][2 * (i & 1)];
        e0[2 * i] = lo2f(c1); e0[2 * i + 1] = hi2f(c1);
      }
      float s0 = 0.f;
#pragma unroll
      for (int i = 0; i < 4; ++i) s0 += lo2f(x0[i]) + hi2f(x0[i]);
      m0 = wave_allsum(s0) * (1.f / D);
#pragma unroll
      for (int i = 0; i < 8; ++i) PIN(e0[i]);
      PIN(m0);
      *LPTR(float, LADDR(L_H2 + 1024) + cw * HROW) = m0;
    }
  }
  u32 a_of = LADDR(L_OP + (2 * ((n >> 2) & 1) + (n & 1)) * OROW + g * 32 + (n & 2) * 8 + (n >> 3) * 2048);
  u32 a_og = a_of + ((n >> 3) ? -2048 : 2048);
  int sidx = (n & 2) ? 0xEEEE : 0x4444;
  f32x4 acc[4];
#pragma unroll
  for (int c = 0; c < 4; ++c) { acc[c] = f32x4{0.f, 0.f, 0.f, 0.f}; PIN(acc[c]); }
  PIN(sidx);
  int vo2 = (in_off(bd, 8 * cw + 2 * (n >> 2) + (g >> 1)) ^ cp) + h * 64 + (n & 3) * 16;
  u32 thv = th;
  u32x4 pv = {0u, th, 0u, th};
  u64 evn = 0x0000ffff0000ffffull;
  asm volatile("" : "+s"(evn));
  PIN(a_og); PIN(pv);
  PIN(a_of); PIN(a_h2w); PIN(vo2); PIN(thv);
  asm volatile("" :: "s"(rt));
  const int pcofs = min(cg0 * 32 + min(lane & 3, ncg - 3) * 64, F - 2);
  u32 pfa = (16 * jj + (lane >> 2)) * F + pcofs;
  PIN(pfa);
  u64 pfs = (u64)(uintptr_t)(cw == 0 ? a.wg : cw == 1 ? a.wu : a.wo + (size_t)h * DH * D);
  asm volatile("" : "+s"(pfs));
  u32 pfo = (cw < 2 ? pfa : 0u) * 2u;
  PIN(pfo);
  {
    u32 a_pr = LADDR(L_PG + cw * 256);
    PIN(a_pr);
    const int lim = 2 * TSTAR - 64 * TAU;
    u32 t0h = t0;
    if (CT && HG) { const u32 tg_ = __builtin_amdgcn_readfirstlane(__hip_atomic_load(s_fl + 8, RLX_WG)); t0h = tg_ ? tg_ - TROFS : t0; }
    int p = 0;
#pragma unroll
    for (int j = JD0; j < 32; ++j) {
      u32 tn, tv; int ta;
      asm volatile("s_cmp_gt_i32 %0, 63\n\ts_cbranch_scc1 3f\n"
                   "1:\n\t"
                   "s_memrealtime vcc\n\tds_read_b32 %2, %4\n\ts_waitcnt lgkmcnt(0)\n\t"
                   "v_readfirstlane_b32 %0, %2\n\ts_sub_u32 %3, vcc_lo, %5\n\ts_lshl_b32 %3, %3, 1\n\t"
                   "s_cmp_gt_i32 %0, 63\n\ts_cbranch_scc1 3f\n\t"
                   "s_cmp_lt_i32 %0, %7\n\ts_cbranch_scc1 2f\n\t"
                   "s_mul_i32 %1, %0, %8\n\ts_sub_i32 %3, %3, %1\n\t"
                   "s_cmp_lt_i32 %3, %6\n\ts_cbranch_scc1 3f\n"
                   "2:\n\ts_sleep 2\n\ts_branch 1b\n"
                   "3:"
                   : "+s"(p), "=&s"(tn), "=&v"(tv), "=&s"(ta) : "v"(a_pr), "s"(t0h), "s"(lim), "i"(PK0 + PKS * (j - JD0)), "i"(TAU) : "memory", "scc", "vcc");
      SB();
      WO_ISSUE(j); SB();
      SB();
    }
  }
  LDS_ARRIVE(s_fl + 13);
  SB();
  LDS_SPIN(s_fl + 13, 8u);
  const u32 pfm = *reinterpret_cast<const GAS u32*>((const GAS unsigned char*)pfs + pfo);
  SB();
  CSTAMP2(21);
  if (cw == 0) STAMP(8);
  BARRIER();
  if (cw == 0) STAMP(9);
  {
    const u32x4 af1 = *LPTR(const u32x4, a_of + 128), ag1 = *LPTR(const u32x4, a_og + 128), af0 = *LPTR(const u32x4, a_of), ag0 = *LPTR(const u32x4, a_og);
    SB();
#define FRAGP(j, c0, c1) do { _Pragma("unroll") for (int c = (c0); c < (c1); ++c) { \
    u32 o_ = (c & 1) ? perm_hi(wr[2 * (j)][c >> 1], wr[2 * (j) + 1][c >> 1]) : perm_lo(wr[2 * (j)][c >> 1], wr[2 * (j) + 1][c >> 1]); wf[c][j] = o_; PIN(wf[c]); } SB(); } while (0)
#pragma unroll
    for (int j = (JD0 - 16) / 2; j < 6; ++j) FRAGP(j, 0, 8);
    float r[4];
    f32x2 tf[4];
#define FOLD0(c) do { tf[c] = fold(acc[c]); SB(); } while (0)
#define FOLD1(c) do { tf[c] += f32x2{tf[c][1], tf[c][0]}; PIN(tf[c]); SB(); } while (0)
#define FOLD2(c) do { r[c] = swap16_add(tf[c][0], tf[c][1]); PIN(r[c]); SB(); } while (0)
#pragma unroll
    for (int i = 0; i < 16; ++i) {
      const int c = (i >> 1) & 3, k = 2 * (i & 1) + (i < 8);
      acc[c] = smfmac(k == 0 ? af0 : k == 1 ? af1 : k == 2 ? ag0 : ag1, wf[8 * (k & 1) + 4 * (k >> 1) + c], acc[c], sidx);
      SB();
      if (i >= 1 && i < 4) FRAGP(6, 3 * (i - 1), i == 3 ? 8 : 3 * i);
      if (i >= 5 && i < 8) FRAGP(7, 3 * (i - 5), i == 7 ? 8 : 3 * (i - 4));
      if (i == 12) { FOLD0(0); FOLD1(0); }
      if (i == 13) FOLD2(0);
      if (i == 14) { FOLD0(1); FOLD1(1); }
    }
    FOLD2(1);
    FOLD0(2);
    pv[0] = cvtpk(r[0], r[1]); PIN(pv);
    SB();
    FOLD1(2); FOLD2(2);
    FOLD0(3); FOLD1(3);
    pv[2] = cvtpk(r[2], swap16_add(tf[3][0], tf[3][1]));
    asm volatile("s_mov_b64 exec, %3\n\tbuffer_store_dwordx4 %0, %1, %2, 0 offen sc1\n\ts_mov_b64 exec, -1\n\ts_nop 0" :: "v"(pv), "v"(vo2), "s"(rt), "s"(evn) : "memory");
  }
  SB();
  if (is_unit) __builtin_amdgcn_s_barrier();
  SB();
  if (cw == 0) STAMP(10);
  const u32 th2 = th ^ 0x80000000u;
  if (is_red) {
    int vi = (in_off(rb, rc) ^ cp) + lane * 8, vx1 = (x1_off(lane >> 3, rb) ^ cp) + (rc * 8 + bq) * 8;
    const u32 s1_ = (n & 2) ? 0u : (n & 1) ? 0x3f800000u : 0x00003f80u;
    u32x4 sel = {s1_, s1_, s1_, s1_};
    int s4 = 0x4444;
    PIN(vi); PIN(vx1); PIN(sel); PIN(s4);
    __builtin_amdgcn_s_sleep(D2);
    u32x8 eg[2];
    f32x4 ya;
    POLL_BEGIN()
#pragma unroll
      for (int i = 0; i < 4; ++i) { const u32x2 v_ = BLD64(rt, vi, i * 512, 16); eg[0][2 * i] = v_[0]; eg[0][2 * i + 1] = v_[1]; }
      ya = f32x4{0.f, 0.f, 0.f, 0.f};
      PIN(ya);
      SB();
      u32 k0 = ne3(eg[0][1], eg[0][3], th);
      PIN(k0);
      SB();
      ya = smfmac(sel, eg[0], ya, s4);
      SB();
      const bool ok = (k0 | ne3(eg[0][5], eg[0][7], th)) == 0u;
    POLL_END(ok)
    STAMPM(11);
    asm volatile("" :: "v"(pfm));
    float y0 = ya[0] + dppf<XOR8>(ya[0]);
    PIN(y0);
    float y1 = ya[1] + dppf<XOR8>(ya[1]);
    PIN(y1);
    y0 = fmaf(e0[2], y0, e0[0]);
    PIN(y0);
    const u32 x1p = cvtpk(y0, fmaf(e0[3], y1, e0[1]));
    __builtin_amdgcn_raw_buffer_store_b64(u32x2{x1p, thv}, rt, vx1, 0, 16);
    SB();
    STAMPM(12);
    const bool pad = lane >= 24;
    const __amdgpu_buffer_rsrc_t rout = MKRS(a.out, B * D * 2);
    int vout = lane < 8 ? (rb * D + rcol) * 2 : OOB;
    float x1a = lo2f(x1p), x1b = hi2f(x1p);
    int u4 = (inx_off(rb, rc) ^ cp) + lane * 8, u5 = pad ? OOB : u4;
    u32 tp = pad ? th2 : 0u;
    PIN(vout); PIN(x1a); PIN(x1b); PIN(u4); PIN(u5); PIN(tp);
    asm volatile("" :: "s"(rout));
    __builtin_amdgcn_s_sleep(D5);
    POLL_BEGIN()
#pragma unroll
      for (int i = 0; i < 6; ++i) { const u32x2 v_ = BLD64(rt, i < 4 ? vi : i < 5 ? u4 : u5, (i & 3) * 512, 16); eg[i >> 2][2 * (i & 3)] = v_[0]; eg[i >> 2][2 * (i & 3) + 1] = v_[1]; }
#pragma unroll
      for (int i = 4; i < 8; ++i) eg[1][i] = 0u;
      ya = f32x4{0.f, 0.f, 0.f, 0.f};
      PIN(ya);
      SB();
      ya = smfmac(sel, eg[0], ya, s4);
      SB();
      u32 k0 = ne3(eg[0][1], eg[0][3], th2) | ne3(eg[0][5], eg[0][7], th2);
      PIN(k0);
      SB();
      ya = smfmac(sel, eg[1], ya, s4);
      SB();
      const bool ok = (k0 | ne3(eg[1][1], eg[1][3] | tp, th2)) == 0u;
    POLL_END(ok)
    STAMPM(13);
    y0 = ya[0] + dppf<XOR8>(ya[0]);
    PIN(y0);
    y1 = ya[1] + dppf<XOR8>(ya[1]);
    PIN(y1);
    y0 = fmaf(e0[4], y0, x1a);
    PIN(y0);
    __builtin_amdgcn_raw_buffer_store_b32(cvtpk(y0, fmaf(e0[5], y1, x1b)), rout, vout, 0, 17);
    STAMPM(14);
    STAMP_DUMP();
    asm volatile("s_endpgm\n\t.fill %0, 4, 0xBF800000" :: "i"(PADB / 4) : "memory");
    return;
  }
  if (!is_unit) {
    if (b >= 3) {
      const int li = min(((hg * 3 + b - 3) * 4 + cw) * 32 + (lane & 31), ncg * 256 - 1);
      const u32 pd_ = *reinterpret_cast<const GAS u32*>((const GAS unsigned char*)a.wd + (u32)(cg0 * 256 + li) * 128u);
      asm volatile("" :: "v"(pd_));
    }
    asm volatile("" :: "v"(pfm));
    if (w == EPW && cw == 3) {
      POLL_BEGIN()
        const bool ok = BLD64(rt, (x1_off(xcd, (lane >> 2) & 7) ^ cp) + (lane & 3) * 512, 0, 16)[1] == th;
        __builtin_amdgcn_s_sleep(24);
      POLL_END(ok)
      if (lane == 0) __hip_atomic_store((GAS u64*)(a.ws + WS_EPOCH), E + 1, RLX_AGENT);
    }
    EXIT();
  }
  const __amdgpu_buffer_rsrc_t rd = MKRS(a.wd + (size_t)cg * 32 * D, 32 * D * 2);
  const int vo_d = g * 8 * (D * 2) + cw * 256 + n * 16;
  const int r4 = 8 * cw + 2 * (n >> 2);
  int vo4 = ((cg < 32 ? in_off(ub + g, r4) : inx_off(ub + g, r4)) ^ cp) + ((cg & 31) ^ (cg < 40 ? 2 * (n >> 2) : 0)) * 64 + (n & 3) * 16;
  u32 a_hr = LADDR((g >> 1) == (n & 1) ? L_HH + (n >> 2) * 64 + (g & 1) * 32 + (n & 2) * 8 : L_FL + 96);
  f32x4 ad[4];
#pragma unroll
  for (int f = 0; f < 4; ++f) { ad[f] = f32x4{0.f, 0.f, 0.f, 0.f}; PIN(ad[f]); }
  u32x4 pw[2] = {{0u, th2, 0u, th2}, {0u, th2, 0u, th2}};
  int vo5 = vo4 ^ 16384;
  PIN(vo4); PIN(vo5); PIN(a_hr); PIN(pw[0]); PIN(pw[1]);
  u32x4 dr[8];
  u32x8 df[4];
  __builtin_amdgcn_s_barrier();
  SB();
#pragma unroll
  for (int e = 0; e < 8; ++e) { dr[e] = BLD(rd, vo_d, e * (D * 2), 0); SB(); }
#define DN_FRAGS() do { _Pragma("unroll") for (int f = 0; f < 4; ++f) \
    _Pragma("unroll") for (int j = 0; j < 4; ++j) { df[f][j] = perm_lo(dr[2 * j][f], dr[2 * j + 1][f]); df[f][4 + j] = perm_hi(dr[2 * j][f], dr[2 * j + 1][f]); } \
    _Pragma("unroll") for (int f = 0; f < 4; ++f) PIN(df[f]); } while (0)
  {
    int vx = (x1_off(xcd, ub + cw) ^ cp) + lane * 16;
    u32 a_sw = LADDR(L_PG + cw * 256 + n * 16 + (g & 1) * 8 + (g >> 1) * 4);
    f32x2 e1[4];
#pragma unroll
    for (int i = 0; i < 4; ++i) { e1[i] = f32x2{e0[2 * i], e0[2 * i + 1]} * -m0; PIN(e1[i]); }
    float s1 = 0.f, s2 = 0.f;
    PIN(vx); PIN(a_sw); PIN(s1); PIN(s2);
    u32x4 xa, xb;
    xa = BLD(rt, vx, 0, 16); xb = BLD(rt, vx, 1024, 16);
    SB();
    DN_FRAGS();
    SB();
    if (__builtin_amdgcn_ballot_w64((ne3(xa[1], xa[3], th) | ne3(xb[1], xb[3], th)) != 0u) != 0ull) {
      POLL_BEGIN()
        xa = BLD(rt, vx, 0, 16); xb = BLD(rt, vx, 1024, 16);
        const bool ok = (ne3(xa[1], xa[3], th) | ne3(xb[1], xb[3], th)) == 0u;
      POLL_END(ok)
    }
    if (cw == 1) STAMP(15);
    const u32 d[4] = {xa[0], xa[2], xb[0], xb[2]};
    f32x2 zz[4];
#pragma unroll
    for (int i = 0; i < 4; ++i) zz[i] = f32x2{lo2f(d[i]), hi2f(d[i])};
    SB();
#pragma unroll
    for (int i = 0; i < 4; ++i) zz[i] = __builtin_elementwise_fma(zz[i], f32x2{e0[2 * i], e0[2 * i + 1]}, e1[i]);
    SB();
    *LPTR(u32, a_h2w) = cvtpk(zz[0][0], zz[0][1]); *LPTR(u32, a_h2w + 16) = cvtpk(zz[1][0], zz[1][1]);
    *LPTR(u32, a_h2w + 512) = cvtpk(zz[2][0], zz[2][1]); *LPTR(u32, a_h2w + 528) = cvtpk(zz[3][0], zz[3][1]);
    SB();
#pragma unroll
    for (int i = 0; i < 4; ++i) { s1 = dot2(d[i], 0x3f803f80u, s1); s2 = dot2(d[i], d[i], s2); }
    PIN(s1); PIN(s2);
    float r = swap32_add(s1, s2);
    r += dppf<XOR1>(r);
    r += dppf<XOR2>(r);
    PIN(r);
    BARRIER();
    r += dppf<XOR7>(r);
    r += dppf<XOR15>(r);
    *LPTR(float, a_sw) = r;
    SB();
    BARRIER();
  }
  asm volatile("" :: "v"(pfm));
  BARRIER();
  {
    const u32x4 af = *LPTR(const u32x4, a_hr);
#pragma unroll
    for (int f = 0; f < 4; ++f) ad[f] = smfmac(af, df[f], ad[f], sidx);
    SB();
#pragma unroll
    for (int f = 0; f < 2; ++f) {
      const f32x2 va = fold(ad[2 * f]), vb = fold(ad[2 * f + 1]);
      pw[f][0] = cvtpk(va[0], va[1]); pw[f][2] = cvtpk(vb[0], vb[1]);
      __builtin_amdgcn_raw_buffer_store_b128(pw[f], rt, f ? vo5 : vo4, 0, 16);
      SB();
    }
  }
  if (cw == 0) STAMP(26);
  EXIT();
}

extern "C" void kernel_launch(void* const* d_in, const int* in_sizes, int n_in, void* d_out, int out_size, void* d_ws, size_t ws_size,
                              hipStream_t stream) {
  Args a;
  a.x = (const u16*)d_in[0]; a.kc = (const u16*)d_in[1]; a.vc = (const u16*)d_in[2]; a.wq = (const u16*)d_in[3]; a.wo = (const u16*)d_in[4];
  a.wg = (const u16*)d_in[6]; a.wu = (const u16*)d_in[7]; a.wd = (const u16*)d_in[8]; a.c = (const u16*)d_in[9]; a.wmod = (const u16*)d_in[10];
  a.out = (u16*)d_out; a.ws = (unsigned char*)d_ws;
  k_block<<<256, 512, 0, stream>>>(a);
}
```

```cpp
#include <hip/hip_runtime.h>
#include <hip/hip_bf16.h>
#include <stdint.h>

typedef unsigned short u16;
typedef unsigned int u32;
typedef unsigned long long u64;
typedef __attribute__((ext_vector_type(2))) unsigned int u32x2;
typedef __attribute__((ext_vector_type(4))) unsigned int u32x4;
typedef __attribute__((ext_vector_type(2))) float f32x2;
typedef __attribute__((ext_vector_type(4))) float f32x4;
typedef __attribute__((ext_vector_type(2))) __bf16 bf16x2_t;
typedef __attribute__((ext_vector_type(8))) __bf16 bf16x8_t;
typedef __attribute__((ext_vector_type(16))) __bf16 bf16x16_t;
typedef __attribute__((ext_vector_type(8))) unsigned int u32x8;
typedef __attribute__((ext_vector_type(16))) unsigned int u32x16;
typedef __attribute__((ext_vector_type(4))) short s16x4;
#define GAS __attribute__((address_space(1)))
#define LAS __attribute__((address_space(3)))
#define RLX_AGENT __ATOMIC_RELAXED, __HIP_MEMORY_SCOPE_AGENT
#define RLX_WG __ATOMIC_RELAXED, __HIP_MEMORY_SCOPE_WORKGROUP
#define SB() __builtin_amdgcn_sched_barrier(0)
#define MEMFENCE() asm volatile("" ::: "memory")
#define PIN(x) asm volatile("" : "+v"(x))

constexpr int B = 8, D = 512, H = 32, SK = 4096, DH = 128, W = 1024, F = 1376, NMOD = 6 * D;

#define PFS 8
#define NPRE 24
#define NQ 8
#define NSM 3
#define PD 9
#define RMP 4
#define NPAUSE 14
#define NPAUSEQ 18
#define NPAUSEM 20
#define PWA 3
#define NWP 14
#define NWP0 8
#define NWM 14
#define NWM0 6
#define NQA 3
#define NWQ 8
#define PB0 3
#define PB1 5
#define PB2 8
#define TQP 850
#define THP 580
#define TAILPF 61
#define B2B 0
#define PSJ 25
#define NWE 8
#define EPW 239
#define NPROBE 1
#define D5 44
#define JD0 22
#define TSTAR 1930
#define TAU 25
#define PK0 36
#define PKS 2
#define D2 4

#define PLK 4
#define PLV 4
#define PLP 5
#define K32 430
#define K64 940
#define K88 1280
#define K99 1445
#define K104 1545
#define K128 1875
#define UOFS 15
#define NOFS 15
#define CEV 4
#define RI 2
#define JS 3
constexpr int WS_EPOCH = 0;
constexpr int WS_RF = 8192;
constexpr int WS_HG = 16384;
constexpr int WS_MG = 32768;
constexpr int WS_QG = 1 << 20;
constexpr int WS_SPAN = 2 << 20;
constexpr int WS_TAIL = 4 << 20;
constexpr int T_IN = 0;
constexpr int T_X1 = 512 << 10;
constexpr int T_PR = 768 << 10;
constexpr int T_CP = 800 << 10;
constexpr int T_INX = 1 << 20;
__device__ __forceinline__ int q_off(int b, int h) { return WS_QG + (b * 2048 + h * 64) * 8; }
__device__ __forceinline__ int mg_off(int b, int k) { return WS_MG + (b * 4 + k) * 2048; }
__device__ __forceinline__ int cls(int x) { return (((x >> 1) & 3) | ((x & 1) << 2)) << 12; }
__device__ __forceinline__ int x1_off(int r, int b) { return T_X1 + (b >> 1) * 32768 + (cls(r) ^ 16384) + (b & 1) * 2048; }
__device__ __forceinline__ int in_off(int b, int rc) { return T_IN + (b * 2 + (rc >> 4)) * 32768 + cls(rc) + ((rc >> 3) & 1) * 2048; }
__device__ __forceinline__ int inx_off(int b, int rc) { return T_INX + b * 32768 + (cls(rc) ^ 4096) + (rc >> 3) * 1024; }
constexpr size_t WS_STAMPS = 8 << 20;
constexpr unsigned SPIN_CAP = 1u << 20;
constexpr int OOB = 0x7ffffff0;

constexpr int L_K = 0;
constexpr int L_MP = 131072;
constexpr int L_PT = L_MP + 16384;
constexpr int L_Q = L_PT + 16 * 68 * 4;
constexpr int L_ML = L_Q + 256;
constexpr int L_OP = L_ML + 64;
constexpr int L_H2 = L_MP;
constexpr int HROW = 1088, OROW = 320;
constexpr int L_HH = L_OP + 5120;
constexpr int L_FL = L_HH + 512;
constexpr int L_ST = L_FL + 128;
constexpr int L_PG = L_ST + 128;
constexpr int L_END = L_PG + 1024;
static_assert(L_END <= 163840, "LDS");

__device__ __forceinline__ float lo2f(u32 x) { return __uint_as_float(x << 16); }
__device__ __forceinline__ float hi2f(u32 x) { return __uint_as_float(x & 0xffff0000u); }
__device__ __forceinline__ u32 cvtpk(float lo, float hi) {
  const bf16x2_t v = __builtin_convertvector(f32x2{lo, hi}, bf16x2_t);
  return __builtin_bit_cast(u32, v);
}
__device__ __forceinline__ float rbf(float x) { return lo2f(cvtpk(x, x)); }
__device__ __forceinline__ float dot2(u32 a, u32 b, float acc) {
  return __builtin_amdgcn_fdot2_f32_bf16(__builtin_bit_cast(bf16x2_t, a), __builtin_bit_cast(bf16x2_t, b), acc, false);
}
__device__ __forceinline__ f32x4 mfma(u32x4 a, u32x4 b, f32x4 c) {
  return __builtin_amdgcn_mfma_f32_16x16x32_bf16(__builtin_bit_cast(bf16x8_t, a), __builtin_bit_cast(bf16x8_t, b), c, 0, 0, 0);
}
__device__ __forceinline__ f32x4 smfmac(u32x4 a, u32x8 b, f32x4 c, int idx) {
  return __builtin_amdgcn_smfmac_f32_16x16x64_bf16(__builtin_bit_cast(bf16x8_t, a), __builtin_bit_cast(bf16x16_t, b), c, idx, 0, 0);
}
__device__ __forceinline__ f32x2 fold(f32x4 v) { return f32x2{v[0], v[1]} + f32x2{v[2], v[3]}; }
__device__ __forceinline__ u32x8 cat(u32x4 a, u32x4 b) { return __builtin_shufflevector(a, b, 0, 1, 2, 3, 4, 5, 6, 7); }
template <int CTRL> __device__ __forceinline__ float dppf(float x) {
  return __builtin_bit_cast(float, __builtin_amdgcn_mov_dpp(__builtin_bit_cast(int, x), CTRL, 0xf, 0xf, true));
}
constexpr int XOR1 = 0xB1, XOR2 = 0x4E, XOR7 = 0x141, XOR15 = 0x140, XOR8 = 0x128;
__device__ __forceinline__ float swap32_add(float a, float b) {
  auto s = __builtin_amdgcn_permlane32_swap(__float_as_uint(a), __float_as_uint(b), false, false);
  return __uint_as_float(s[0]) + __uint_as_float(s[1]);
}
__device__ __forceinline__ float swap16_add(float a, float b) {
  auto s = __builtin_amdgcn_permlane16_swap(__float_as_uint(a), __float_as_uint(b), false, false);
  return __uint_as_float(s[0]) + __uint_as_float(s[1]);
}
__device__ __forceinline__ float half_allsum(float x) {
  x += dppf<XOR1>(x); x += dppf<XOR2>(x); x += dppf<XOR7>(x); x += dppf<XOR15>(x);
  return swap16_add(x, x);
}
__device__ __forceinline__ float wave_allsum(float x) { x = half_allsum(x); return swap32_add(x, x); }
__device__ __forceinline__ float wave_allmax(float x) {
  x = fmaxf(x, dppf<XOR1>(x)); x = fmaxf(x, dppf<XOR2>(x)); x = fmaxf(x, dppf<XOR7>(x)); x = fmaxf(x, dppf<XOR15>(x));
  auto s = __builtin_amdgcn_permlane16_swap(__float_as_uint(x), __float_as_uint(x), false, false);
  x = fmaxf(__uint_as_float(s[0]), __uint_as_float(s[1]));
  auto u = __builtin_amdgcn_permlane32_swap(__float_as_uint(x), __float_as_uint(x), false, false);
  return fmaxf(__uint_as_float(u[0]), __uint_as_float(u[1]));
}
template <int CTRL, int BIT> __device__ __forceinline__ float tr_row(float lo, float hi, int lane) {
  const float t1 = lo + dppf<CTRL>(lo), t2 = hi + dppf<CTRL>(hi);
  return (lane & BIT) ? t2 : t1;
}
__device__ __forceinline__ float row_tr_reduce16(float (&x)[16], int lane) {
#pragma unroll
  for (int i = 0; i < 8; ++i) x[i] = tr_row<XOR8, 8>(x[i], x[i + 8], lane);
#pragma unroll
  for (int i = 0; i < 4; ++i) x[i] = tr_row<XOR7, 4>(x[i], x[i + 4], lane);
#pragma unroll
  for (int i = 0; i < 2; ++i) x[i] = tr_row<XOR2, 2>(x[i], x[i + 2], lane);
  return tr_row<XOR1, 1>(x[0], x[1], lane);
}
__device__ __forceinline__ u32 perm_lo(u32 a, u32 b) { return __builtin_amdgcn_perm(b, a, 0x05040100u); }
__device__ __forceinline__ u32 perm_hi(u32 a, u32 b) { return __builtin_amdgcn_perm(b, a, 0x07060302u); }
__device__ __forceinline__ void to_frags(u32x4* L) {
  u32x4 o[8];
#pragma unroll
  for (int c = 0; c < 8; ++c)
#pragma unroll
    for (int j = 0; j < 4; ++j) o[c][j] = (c & 1) ? perm_hi(L[2 * j][c >> 1], L[2 * j + 1][c >> 1]) : perm_lo(L[2 * j][c >> 1], L[2 * j + 1][c >> 1]);
#pragma unroll
  for (int c = 0; c < 8; ++c) { L[c] = o[c]; PIN(L[c]); }
}
__device__ __forceinline__ void to_frags2(const u32x4* L, u32x8* o) {
#pragma unroll
  for (int c = 0; c < 8; ++c) {
#pragma unroll
    for (int j = 0; j < 8; ++j) o[c][j] = (c & 1) ? perm_hi(L[2 * j][c >> 1], L[2 * j + 1][c >> 1]) : perm_lo(L[2 * j][c >> 1], L[2 * j + 1][c >> 1]);
  }
#pragma unroll
  for (int c = 0; c < 8; ++c) PIN(o[c]);
}
__device__ __forceinline__ float sel4(float a0, float a1, float a2, float a3, int g) {
  const float x = (g & 1) ? a1 : a0, y = (g & 1) ? a3 : a2;
  return (g & 2) ? y : x;
}
__device__ __forceinline__ u32 ne3(u32 a, u32 b, u32 c) { return __builtin_amdgcn_bitop3_b32(a, b, c, 0x7E); }
__device__ __forceinline__ bool ok2(u32x4 v, u32 th) { return (v[1] == th) & (v[3] == th); }

#define BLD(rs, vo, so, AUX) __builtin_amdgcn_raw_buffer_load_b128((rs), (vo), (so), (AUX))
#define BLD64(rs, vo, so, AUX) __builtin_amdgcn_raw_buffer_load_b64((rs), (vo), (so), (AUX))
#define BLD32(rs, vo, so, AUX) __builtin_amdgcn_raw_buffer_load_b32((rs), (vo), (so), (AUX))
#define PUBLISH(pay, rs, vo, so) __builtin_amdgcn_raw_buffer_store_b64((u32x2{(pay), th}), (rs), (vo), (so), 16)
#define MKRS(p, n) __builtin_amdgcn_make_buffer_rsrc((void*)(p), 0, (n), 0x00020000)
#define VMWAIT(n) asm volatile("s_waitcnt vmcnt(%0)" :: "i"(n) : "memory")
#define BARRIER() do { asm volatile("s_waitcnt lgkmcnt(0)\n\ts_barrier" ::: "memory"); SB(); } while (0)
#define LDSWAIT() asm volatile("s_waitcnt lgkmcnt(0)" ::: "memory")
#define LADDR(off) ((u32)(uintptr_t)(LAS unsigned char*)lds + (u32)(off))
#define LPTR(T, a) ((LAS T*)(uintptr_t)(a))
#define STAMP(k) do {} while (0)
#define STAMPM(k) do {} while (0)
#define STAMP_DUMP() do {} while (0)
#define REF_STAMP() do {} while (0)
#define EXIT() do { STAMP_DUMP(); return; } while (0)
#define POLL_BEGIN() { unsigned spin_ = SPIN_CAP; u64 more_; _Pragma("nounroll") do { u64 lim_; asm volatile("s_sub_u32 %0, %0, 1\n\ts_cselect_b64 %1, 0, -1" : "+s"(spin_), "=s"(lim_) :: "memory", "scc");
#define POLL_END(ok) more_ = __builtin_amdgcn_ballot_w64(!(ok)) & lim_; } while (more_ != 0ull); }
#define LDS_SPIN(ptr, n) do { for (unsigned spin_ = 0; __hip_atomic_load((ptr), RLX_WG) < (n) && spin_ < (SPIN_CAP << 3); ++spin_) __builtin_amdgcn_s_sleep(1); MEMFENCE(); } while (0)
#define LDS_ARRIVE(ptr) do { LDSWAIT(); if (lane == 0) __hip_atomic_fetch_add((ptr), 1u, RLX_WG); } while (0)

struct Args {
  const u16* x; const u16* kc; const u16* vc; const u16* wq; const u16* wo; const u16* c; const u16* wmod; unsigned char* ws;
  const u16* wg; const u16* wu; const u16* wd; u16* out;
};

constexpr int N1 = NPRE + NQ, N2 = N1 + NSM;
constexpr int issued(int i) { return i < 0 ? N2 : ((N2 + i / RMP > i + PD + 1 ? N2 + i / RMP : i + PD + 1) > 64 ? 64 : (N2 + i / RMP > i + PD + 1 ? N2 + i / RMP : i + PD + 1)); }
static_assert(NWP0 <= NWP && NWM <= NWP && NWM0 <= NWP && NWQ <= NWP && NWE <= NWP && NQA <= NWQ, "");
static_assert(NWP <= JD0 && JD0 >= 16 && JD0 <= 28 && JD0 % 2 == 0, "");
static_assert((EPW & 63) == 47, "EPW: neither an MLP unit nor a producer");
constexpr int sched(int i) { return (i < 64 ? K32 + (K64 - K32) * (i - 32) / 32 : i < 88 ? K64 + (K88 - K64) * (i - 64) / 24 : i < 99 ? K88 + (K99 - K88) * (i - 88) / 11 : i < 104 ? K99 + (K104 - K99) * (i - 99) / 5 : K104 + (K128 - K104) * (i - 104) / 24); }
#define WAIT_T(dt) do { while ((int)((u32)__builtin_amdgcn_s_memrealtime() - t0) < (dt)) __builtin_amdgcn_s_sleep(2); } while (0)
constexpr float SCALE = 0.08838834764831845f;

#define PADA 0x300
#define PADB 0xe04
#define PADC 0x60
__device__ __attribute__((used, noinline, aligned(4096))) void k_filler() { asm volatile(".fill %0, 4, 0xBF800000" :: "i"(PADA / 4 - 16)); }

__global__ void __launch_bounds__(512, 2) k_block(Args a) {
  __shared__ __attribute__((aligned(16))) unsigned char lds[L_END];
  const int w = blockIdx.x, t = threadIdx.x;
  const int wave = __builtin_amdgcn_readfirstlane(t >> 6), lane = t & 63, g = lane >> 4, n = lane & 15;
  const int xcd = w & 7, jj = w >> 3;
  u32 pfc = 0;
  {
    u64 pc; int len;
    asm volatile("s_getpc_b64 %0\n.Lkb_pf_base:\n\ts_mov_b32 %1, .Lkb_pf_end-.Lkb_pf_base+128" : "=s"(pc), "=s"(len));
    const int lo = (jj * 9 + min(lane, 8)) * 128;
    if (wave == 1) pfc = *reinterpret_cast<const GAS u32*>(pc + (lo < len ? lo : 0));
    SB();
  }
  const int b = jj & 7, hg = jj >> 3;
  const int h = 4 * xcd + (hg ^ (xcd & 3)), bd = b ^ xcd;
  const size_t kvoff = ((size_t)(bd * H + h) * SK + (SK - W)) * DH;
  int vo_dma = (wave & 3) * 1024 + g * 256 + ((n ^ (4 * (wave & 3) + g)) << 4);
  const u32 l0 = LADDR(L_K + (wave & 3) * 1024);
  int vo = (wave & 3) * 1024 + lane * 16;
  u32 kb = (u32)kvoff * 2u, so0 = (wave >> 2) * 4096, keep0, rkn = W * DH * 2, rkf = 0x00020000;
  PIN(vo); PIN(vo_dma);
  asm volatile("s_mov_b32 %0, m0\n\ts_add_u32 m0, %5, %2" : "=&s"(keep0), "+s"(kb), "+s"(so0), "+s"(rkn), "+s"(rkf) : "s"(l0) : "scc");
  SB();
  const bool pre_ = wave < 4 && (xcd & 1) && ((0xD0 >> b) & 1);
  u32x4 cf[2], xr, xr2; u32 xj;
#define GLD(T, p, o) (*reinterpret_cast<const GAS T*>((const GAS unsigned char*)(p) + (o)))
  u32 pf1 = 0;
  if (wave < 4 && (xcd & 1) && (b == 5 || (wave == 0 && b < 4))) {
    const int un = hg * 4 + (b == 5 ? 16 + wave : b);
    u32 op_ = ((16 * un + (lane >> 2)) * NMOD + 128 * (xcd >> 1) + 64 * (lane & 1) + 512 * ((lane >> 1) & 1)) * 2;
    PIN(op_); SB();
    pf1 = GLD(u32, a.wmod, op_);
    SB();
  }
  const u64 kp = (u64)(uintptr_t)a.kc + kb;
  u32x4 rk4 = {(u32)kp, (u32)(kp >> 32) & 0xffffu, rkn, rkf};
  {
    asm volatile("buffer_load_dwordx4 %2, %3, %1 offen nt lds\n\t"
                 "s_add_u32 m0, m0, 0x2000\n\t" "s_add_u32 %1, %1, 0x2000\n\t"
                 "buffer_load_dwordx4 %2, %3, %1 offen nt lds\n\t"
                 "s_add_u32 m0, m0, 0x2000\n\t" "s_add_u32 %1, %1, 0x2000\n\t"
                 "buffer_load_dwordx4 %2, %3, %1 offen nt lds\n\t"
                 "s_add_u32 m0, m0, 0x2000\n\t" "s_add_u32 %1, %1, 0x2000\n\t"
                 "buffer_load_dwordx4 %2, %3, %1 offen nt lds\n\t"
                 "s_mov_b32 m0, %0"
                 : "+s"(keep0), "+s"(so0) : "v"(vo_dma), "s"(rk4) : "memory", "scc");
    SB();
  }
  const __amdgpu_buffer_rsrc_t rk = MKRS(kp, W * DH * 2);
  const u32 t0 = (u32)__builtin_amdgcn_s_memrealtime();
  SB();
  asm volatile("" :: "s"(rk4));
  if (pre_) {
    u32 oc_ = ((n & 7) * D + 128 * wave + 32 * (n >> 3) + 8 * g) * 2, ox_ = (2 * wave * D + lane * 16) * 2;
    u32 oj_ = ((lane & 7) * D + ((32 * (4 * (xcd >> 1) + hg) + 8 * ((lane >> 3) & 3) + 2 * wave) & (D - 1))) * 2;
    PIN(oc_); PIN(ox_); PIN(oj_); SB();
    cf[0] = GLD(u32x4, a.c, oc_); cf[1] = GLD(u32x4, a.c + 64, oc_);
    xr = GLD(u32x4, a.x, ox_); xr2 = GLD(u32x4, a.x + 8, ox_); xj = GLD(u32, a.x, oj_);
    SB();
  }
  const int xe = xcd >> 1;
  const bool odd = xcd & 1;
  const int ncg = odd && xcd != 7 ? 6 : 5;
  const int ui = 3 * hg + b;
  const bool is_unit = b < 3 && ui < 2 * ncg;
  const int cg0 = 5 * xcd + xe, cg = cg0 + (ui >> 1), ub = 4 * (ui & 1);
  const bool is_red = b >= 6;
  const int rb = 2 * hg + (b & 1), rc = xcd + 8 * (wave & 3);
  const bool is_urg = odd && b == 7;
  const bool is_prod = !odd && b >= 4;
  const bool is_mod = odd && (b == 4 || b == 6);
  const bool has_tile = is_urg || is_prod || is_mod;
  const __amdgpu_buffer_rsrc_t rw = MKRS(a.ws, WS_SPAN);
  unsigned char* const tw = (unsigned char*)(((uintptr_t)a.ws + WS_TAIL + (2 << 20) - 1) & ~(uintptr_t)((2 << 20) - 1));
  const __amdgpu_buffer_rsrc_t rt = MKRS(tw, 2 << 20);
  u32* s_fl = reinterpret_cast<u32*>(lds + L_FL);
  float* s_ml = reinterpret_cast<float*>(lds + L_ML);
  float* s_pt = reinterpret_cast<float*>(lds + L_PT);
  if (t < 32 + 32 + 256) s_fl[t] = 0u;

  if (wave >= 4) {
    const int sw = wave - 4;
    const __amdgpu_buffer_rsrc_t rv = MKRS(a.vc + kvoff, W * DH * 2);
    BARRIER();
    {
      u32 keep, so = 8 * 4096;
#define DMA_LOOP(END) asm volatile("s_nop 4\n\t" \
                   "s_mov_b32 %0, m0\n\t" \
                   "s_add_u32 m0, %4, %1\n\t" \
                   "s_nop 0\n" \
                   "1:\n\t" \
                   "buffer_load_dwordx4 %2, %3, %1 offen nt lds\n\t" \
                   "s_add_u32 %1, %1, 0x1000\n\t" \
                   "s_add_u32 m0, m0, 0x1000\n\t" \
                   "s_waitcnt vmcnt(%5)\n\t" \
                   "s_cmp_lt_u32 %1, %6\n\t" \
                   "s_cbranch_scc1 1b\n\t" \
                   "s_mov_b32 m0, %0" \
                   : "=&s"(keep), "+s"(so) : "v"(vo_dma), "s"(rk), "s"(l0), "i"(PFS - 1), "s"((END) * 4096) : "memory", "scc")
      if (has_tile) { DMA_LOOP(is_mod ? NPAUSEM : is_prod ? NPAUSEQ : NPAUSE); BARRIER(); BARRIER(); }
      DMA_LOOP(32);
    }
    SB();
    if (sw == 0) STAMP(28);
    u32x4 kr[32], vr[64];
    u32 a_pg = LADDR(L_PG + sw * 256 + lane * 4);
    PIN(a_pg);
    u64 tn, re;
    u32x16 rs;
    u32 tref = t0 - (1u << 30);
    int dlag = 1 << 29;
#define CLK() asm volatile("s_memrealtime %0" : "=s"(tn))
#define SAMPLE(i) do { if (((i) % CEV) == 0) { asm volatile("s_waitcnt lgkmcnt(0)" : "+s"(tn)); dlag = (int)((u32)tn - tref); \
      asm volatile("s_memrealtime %0" : "=s"(tn) : "s"(dlag)); } } while (0)
#define SREF(i) sched(((i) / CEV) * CEV - 1)
#define CTRL(i, HI, LO) do { SAMPLE(i); \
      asm volatile("s_cmp_ge_i32 %0, %3\n\ts_cbranch_scc1 1f\n\ts_waitcnt vmcnt(%2)\n1:\n\ts_waitcnt vmcnt(%1)" :: "s"(dlag), "i"((HI) - 1), "i"((LO) - 1), "i"(SREF(i)) : "memory", "scc"); } while (0)
#define CTRLP(i, LO) do { SAMPLE(i); \
      asm volatile("s_cmp_ge_i32 %0, %2\n\ts_cbranch_scc1 1f\n\ts_waitcnt vmcnt(%1)\n1:" :: "s"(dlag), "i"((LO) - 1), "i"(SREF(i)) : "memory", "scc"); } while (0)
    asm volatile("s_load_dwordx16 %0, %2, %3\n\ts_load_dwordx2 %1, %2, %4" : "=&s"(rs), "=&s"(re) : "s"(a.ws), "i"(WS_RF), "i"(WS_EPOCH) : "memory");
#define VISSUE(i) do { vr[i] = BLD(rv, vo, (i) * 4096, 2); if ((i) + 1 >= PK0) { SB(); *LPTR(volatile u32, a_pg) = (i) + 1; } } while (0)
#pragma unroll
    for (int i = 0; i < 32; ++i) { kr[i] = BLD(rk, vo, 131072 + i * 4096, 2); SB();
      if (i == RI) {
        asm volatile("s_waitcnt lgkmcnt(0)" : "+s"(rs), "+s"(re));
        const u32 tg = (u32)re + 1u;
        int sum = 0, cnt = 0;
#pragma unroll
        for (int k = 0; k < 8; ++k) { const bool v_ = rs[2 * k + 1] == tg; sum += v_ ? (int)(rs[2 * k] - t0) : 0; cnt += v_; }
        const u32 tr_ = t0 + (u32)(int)((float)sum * __builtin_amdgcn_rcpf((float)cnt));
        tref = __builtin_amdgcn_readfirstlane(cnt ? tr_ : tref);
        REF_STAMP();
        tref += is_unit ? -UOFS : NOFS;
        CLK();
        SB();
      }
      if (i > RI) CTRL(32 + i, PFS, PLK); else VMWAIT(PFS - 1);
      if (i == PFS - 1) LDS_ARRIVE(s_fl + 5); SB(); }
    if (sw == 0) STAMP(29);
#pragma unroll
    for (int i = 0; i < NPRE; ++i) { VISSUE(i); SB(); if (i + 1 < NPRE) CTRL(64 + i, PFS, PLV); SB(); }
    asm volatile("s_waitcnt lgkmcnt(0)" : "+s"(tn));
    if (sw == 0) STAMP(16);
    LDS_SPIN(s_fl + 6, 1u);
    if (sw == 0 && w != EPW) STAMP(17);
    const u32x4 qp = *reinterpret_cast<const u32x4*>(lds + L_Q + n * 16);
    float sv[2];
#pragma unroll
    for (int G = 0; G < 2; ++G) {
      float xx[16];
#pragma unroll
      for (int c = 0; c < 16; ++c) {
        const int i = 16 * G + c;
        SB();
        float d = dot2(kr[i][0], qp[0], 0.f); d = dot2(kr[i][1], qp[1], d); d = dot2(kr[i][2], qp[2], d); d = dot2(kr[i][3], qp[3], d);
        xx[c] = d; PIN(xx[c]);
        SB();
        if ((i + 1) * NQ / 32 > i * NQ / 32) VISSUE(NPRE + i * NQ / 32);
      }
      SB();
      sv[G] = rbf(rbf(row_tr_reduce16(xx, lane)) * SCALE);
    }
    s_pt[(sw * 4 + g) * 68 + 32 + n] = sv[0];
    s_pt[(sw * 4 + g) * 68 + 48 + n] = sv[1];
    {
      const float mw = wave_allmax(fmaxf(sv[0], sv[1]));
      const float lw = wave_allsum(__expf(sv[0] - mw) + __expf(sv[1] - mw));
      if (lane == 0) *reinterpret_cast<f32x2*>(s_ml + wave * 2) = f32x2{mw, lw};
    }
    LDS_ARRIVE(s_fl + 7);
    SB();
#pragma unroll
    for (int i = N1; i < N2 - 1; ++i) { VISSUE(i); SB(); }
    if (sw == 0 && w != EPW) STAMP(18);
    LDS_SPIN(s_fl + 7, 8u);
    {
      const f32x4 m0 = *reinterpret_cast<const f32x4*>(s_ml), m1 = *reinterpret_cast<const f32x4*>(s_ml + 4);
      const f32x4 m2 = *reinterpret_cast<const f32x4*>(s_ml + 8), m3 = *reinterpret_cast<const f32x4*>(s_ml + 12);
      f32x4* pp = reinterpret_cast<f32x4*>(s_pt + (sw * 4 + g) * 68 + 4 * n);
      const f32x4 s4 = *pp;
      const float m = fmaxf(fmaxf(fmaxf(m0[0], m0[2]), fmaxf(m1[0], m1[2])), fmaxf(fmaxf(m2[0], m2[2]), fmaxf(m3[0], m3[2])));
      const float l = ((m0[1] * __expf(m0[0] - m) + m0[3] * __expf(m0[2] - m)) + (m1[1] * __expf(m1[0] - m) + m1[3] * __expf(m1[2] - m))) +
                      ((m2[1] * __expf(m2[0] - m) + m2[3] * __expf(m2[2] - m)) + (m3[1] * __expf(m3[0] - m) + m3[3] * __expf(m3[2] - m)));
      const float il = 1.f / l;
      u32x4 p4;
#pragma unroll
      for (int i = 0; i < 4; ++i) { const float p = __expf(s4[i] - m) * il; p4[i] = cvtpk(p, p); }
      *reinterpret_cast<u32x4*>(pp) = p4;
      SB();
      VISSUE(N2 - 1);
      SB();
      CLK(); dlag = 1 << 29;
      LDSWAIT();
    }
    if (sw == 0 && (w & 63) != 47) STAMP(19);
    const __amdgpu_buffer_rsrc_t rg = MKRS(a.wg + cg * 32, is_unit ? D * F * 2 - cg * 64 : 0), ru = MKRS(a.wu + cg * 32, is_unit ? D * F * 2 - cg * 64 : 0);
    int vo_g = (128 * sw + 8 * g + (n >> 2)) * (F * 2) + ((lane & 3) ^ (lane >> 5)) * 16, vo_u = vo_g ^ 32;
    u32 a_op = LADDR(L_OP + sw * OROW + (n >> 1) * 32 + (2 * (n & 1) + (g & 1)) * 4 + (g >> 1) * 2048);
    u32 a_rr = LADDR(L_K + ((lane >> 1) & 1) * 32768 + (8 * g + (n >> 2)) * 64 + (sw ^ ((g & 1) + (lane & 2))) * 16 + (lane & 1) * 8);
    PIN(a_op); PIN(a_rr); PIN(vo_g); PIN(vo_u);
    asm volatile("" :: "s"(rg), "s"(ru));
    u32 msk[4];
#pragma unroll
    for (int j = 0; j < 4; ++j) msk[j] = ((n >> 1) == j) ? ((n & 1) ? 0xffff0000u : 0x0000ffffu) : 0u;
    f32x4 oacc = {0.f, 0.f, 0.f, 0.f};
    const unsigned char* prow = lds + L_PT + (sw * 4 + g) * 68 * 4;
    u32x4 pq[2];
    pq[0] = *reinterpret_cast<const u32x4*>(prow);
#define PV1(I) { constexpr int i = (I); \
      SB(); \
      if ((i & 3) == 0 && i + 4 < 64) pq[((i >> 2) + 1) & 1] = *reinterpret_cast<const u32x4*>(prow + (i + 4) * 4); \
      const u32 pd = pq[(i >> 2) & 1][i & 3]; \
      const u32x4 af = {pd & msk[0], pd & msk[1], pd & msk[2], pd & msk[3]}; \
      oacc = mfma(af, vr[i], oacc); \
      SB(); \
      static_assert(issued(i) - issued(i - 1) <= 1, ""); \
      if (issued(i) > issued(i - 1)) { constexpr int j = issued(i - 1) < 64 ? issued(i - 1) : 63; CTRLP(64 + j, PLP); SB(); VISSUE(j); SB(); } \
      if (i == TAILPF) { LDS_ARRIVE(s_fl + 13); SB(); } }
#define PV4(I) PV1(I) PV1((I) + 1) PV1((I) + 2) PV1((I) + 3)
#define PV16(I) PV4(I) PV4((I) + 4) PV4((I) + 8) PV4((I) + 12)
    PV16(0) PV16(16) PV16(32) PV16(48)
    asm volatile("s_waitcnt lgkmcnt(0)" : "+s"(tn));
    SB();
    *LPTR(u32, a_op) = cvtpk(oacc[0], oacc[1]); *LPTR(u32, a_op + 16) = cvtpk(oacc[2], oacc[3]);
    if (sw == 0) STAMP(20);
    BARRIER();
    if (!is_unit) EXIT();
    {
      u32 keep;
      asm volatile("s_mov_b32 %0, m0" : "=s"(keep));
#pragma unroll
      for (int e = 0; e < 16; ++e) {
        if (e == B2B) { __builtin_amdgcn_s_barrier(); SB(); }
        const int it = (e & 3) + 4 * (e >> 3), mt = (e >> 2) & 1;
        asm volatile("s_add_u32 m0, %2, %3\n\ts_nop 0\n\tbuffer_load_dwordx4 %0, %1, %4 offen lds"
                     :: "v"(mt ? vo_u : vo_g), "s"(mt ? ru : rg), "s"(LADDR(L_K) + sw * 8192), "i"(mt * 32768 + it * 1024), "s"((32 * (it >> 1) + 4 * (it & 1)) * (F * 2)) : "memory", "scc");
        SB();
      }
      asm volatile("s_mov_b32 m0, %0" :: "s"(keep));
    }
    __builtin_amdgcn_s_barrier();
    SB();
    if (sw == 0) STAMP(1);
    u32x8 gf[8];
#define GF_READ(s) do { _Pragma("unroll") for (int u = 0; u < 4; ++u) { \
      const u32x2 t_ = __builtin_bit_cast(u32x2, __builtin_amdgcn_ds_read_tr16_b64_v4i16(LPTR(s16x4, a_rr + (s) * 4096 + (u >> 1) * 2048 + (u & 1) * 256))); \
      gf[s][2 * u] = t_[0]; gf[s][2 * u + 1] = t_[1]; } } while (0)
#define LDS_SPIN0(ptr, n) do { for (unsigned spin_ = 0; __hip_atomic_load((ptr), RLX_WG) < (n) && spin_ < (SPIN_CAP << 3); ++spin_) {} MEMFENCE(); } while (0)
    int sidx = (n & 2) ? 0xEEEE : 0x4444;
    f32x4 acc = {0.f, 0.f, 0.f, 0.f}, acs = {0.f, 0.f, 0.f, 0.f}, acd = {0.f, 0.f, 0.f, 0.f};
    PIN(sidx); PIN(acc); PIN(acs); PIN(acd);
    u32 a_h2 = LADDR(L_H2 + (n >> 2) * HROW + g * 32 + (n & 2) * 8);
    u32 a_hw = LADDR(L_HH + (n >> 3) * 256 + g * 64 + (sw >> 1) * 32 + ((n >> 1) & 1) * 16 + (2 * (sw & 1) + ((n >> 2) & 1)) * 4 + (n & 1) * 2);
    u32 a_st = LADDR(L_PG + g * 256 + g * 16), a_z = a_h2 + (n & 1) * 128;
    PIN(a_h2); PIN(a_hw); PIN(a_st); PIN(a_z);
    constexpr float L2E = 1.4426950408889634f;
    float kk = n < 8 ? L2E : 1.f, mz = *LPTR(const float, LADDR(L_H2 + 1024) + g * HROW);
    float kd0 = n < 8 ? 1.f / (D * L2E) : 1.f / D, kd1 = n < 8 ? 1.f / (D * L2E * L2E) : 1.f / D, ke = n < 8 ? 1e-5f / (L2E * L2E) : 1e-5f;
    PIN(kk); PIN(kd0); PIN(kd1); PIN(ke);
    u32x4 af[8];
#pragma unroll
    for (int s = 0; s < 8; ++s) af[s] = *LPTR(const u32x4, a_h2 + (4 + (n >> 2) + (n & 1)) * HROW + s * 128);
    SB();
    PIN(mz);
    VMWAIT(8);
    LDS_ARRIVE(s_fl + 4);
    LDS_SPIN0(s_fl + 4, 4u);
#pragma unroll
    for (int s = 0; s < 8; s += 2) GF_READ(s);
    SB();
#pragma unroll
    for (int s = 0; s < 8; s += 2) acs = smfmac(af[s], gf[s], acs, sidx);
    SB();
    VMWAIT(0);
    if (sw == 0) STAMP(31);
    LDS_ARRIVE(s_fl + 3);
    LDS_SPIN0(s_fl + 3, 4u);
#pragma unroll
    for (int s = 1; s < 8; s += 2) GF_READ(s);
    SB();
#pragma unroll
    for (int s = 1; s < 8; s += 2) acs = smfmac(af[s], gf[s], acs, sidx);
    PIN(acs);
    SB();
    if (sw == 0) STAMP(24);
    BARRIER();
    if (sw == 0) STAMP(25);
    {
      u32x4 az[4];
#pragma unroll
      for (int s = 0; s < 4; ++s) az[s] = *LPTR(const u32x4, a_z + s * 256);
      SB();
      f32x2 cs = fold(acs);
      cs[1] *= kk;
      PIN(cs);
      SB();
      f32x4 st;
      float s1, s2, rs, dm;
#pragma unroll
      for (int s = 0; s < 8; ++s) {
        const int k = s == 5 ? 6 : s == 6 ? 5 : s;
        if (k & 1) acd = smfmac(az[k >> 1], gf[k], acd, sidx); else acc = smfmac(az[k >> 1], gf[k], acc, sidx);
        SB();
        if (s == 0) {
          asm volatile("s_barrier" ::: "memory");
          SB();
          st = *LPTR(const f32x4, a_st);
        }
        if (s == 3) { s1 = st[0] + st[2]; PIN(s1); s2 = st[1] + st[3]; PIN(s2); }
        if (s == 4) { rs = s1 * kd0; PIN(rs); s2 = fmaf(s2, kd1, ke); PIN(s2); }
        if (s == 5) { rs = fmaf(-rs, rs, s2); asm volatile("v_max_f32 %0, %0, %1" : "+v"(rs) : "v"(ke)); }
        if (s == 6) { rs = __builtin_amdgcn_rsqf(rs); PIN(rs); }
        if (s == 7) { dm = fmaf(s1, -1.f / D, mz) * cs[0]; PIN(dm); dm = fmaf(rs, dm, cs[1]); PIN(dm); }
        SB();
      }
      float u_ = fmaf(rs, acc[0] + acc[2], dm);
      PIN(u_);
      SB();
      const float gv = fmaf(rs, acd[1] + acd[3], u_);
      const float hv = dppf<XOR8>(gv) * __builtin_amdgcn_rcpf(1.f + __builtin_amdgcn_exp2f(-gv));
      *LPTR(u16, a_hw) = (u16)cvtpk(hv, hv);
    }
    BARRIER();
    asm volatile(".Lkb_pf_end:");
    EXIT();
  }

  const int cw = wave;
  const bool prb = (w & 63) == (EPW & 63);
  if (prb) {
#pragma unroll
    for (int i = 0; i < NPROBE; ++i) __builtin_amdgcn_raw_buffer_store_b128(u32x4{0u, 0u, 0u, 0u}, rt, T_PR + (hg * 4 + cw) * 128 + (lane >> 3) * 4096 + (lane & 7) * 16, i * 2048, 16);
    SB();
  }
  const u64 E = __hip_atomic_load((GAS u64*)(a.ws + WS_EPOCH), RLX_AGENT);
  u32 pf0 = 0, pf2 = 0;
  {
    u64 ps = (u64)(uintptr_t)(odd ? a.wmod + 1024 + 512 * xe : a.wq + 1024 * xe);
    u32 po = (8 * ((hg * 4 + b) * 4 + cw) + (lane >> 3)) * (odd ? NMOD * 2 : H * DH * 2) + 128 * (lane & 7);
    asm volatile("" : "+s"(ps));
    PIN(po); PIN(pf0); PIN(pf2);
    SB();
    if (b < 4) {
      pf0 = GLD(u32, ps, po);
      if (!odd) pf2 = GLD(u32, ps + 1024, po);
    }
  }
  SB();
  u32 th;
#define GET_TAG() do { th = __builtin_amdgcn_readfirstlane((u32)(E + 1)); } while (0)
  u32x4 wr[32];
  const int bq = lane & 7, ch8 = n & 7;
  const int trow = 128 * cw + 32 * (n >> 3) + 8 * g;
  float* s_mp = reinterpret_cast<float*>(lds + L_MP);
#define TILE_ISSUE(RS, VO, STRIDE) do { BARRIER(); _Pragma("unroll") for (int i = 0; i < 16; ++i) { wr[i] = BLD(RS, VO, (64 * (i >> 3) + (i & 7)) * (STRIDE), 0); SB(); } BARRIER(); } while (0)
#define TILE_MFMA(A0, A1) do { f32x4 acc_[8]; \
    _Pragma("unroll") for (int c = 0; c < 8; ++c) acc_[c] = mfma(A0, wr[c], f32x4{0.f, 0.f, 0.f, 0.f}); \
    _Pragma("unroll") for (int c = 0; c < 8; ++c) acc_[c] = mfma(A1, wr[8 + c], acc_[c]); \
    if ((g >> 1) == (n >> 3)) { _Pragma("unroll") for (int c = 0; c < 8; ++c) *reinterpret_cast<f32x4*>(s_mp + (cw * 2 + (n >> 3)) * 512 + (c * 8 + ch8) * 8 + 4 * (g & 1)) = acc_[c]; } \
    LDS_ARRIVE(s_fl); LDS_SPIN(s_fl, 4u); } while (0)
#define TILE_SUM2(C, CH, PAIR) do { f32x2 v_ = {0.f, 0.f}; \
    _Pragma("unroll") for (int p = 0; p < 8; ++p) v_ += *reinterpret_cast<const f32x2*>(s_mp + p * 512 + ((C) * 8 + (CH)) * 8 + 2 * cw); \
    auto s_ = __builtin_amdgcn_permlane32_swap(__float_as_uint(v_[0]), __float_as_uint(v_[1]), false, false); \
    PAIR = cvtpk(__uint_as_float(s_[0]), __uint_as_float(s_[1])); } while (0)
  const __amdgpu_buffer_rsrc_t ro = MKRS(a.wo + (size_t)h * DH * D, DH * D * 2);
  const int vo_o = g * 8 * (D * 2) + cw * 256 + n * 16;
#define WO_ISSUE(j) wr[((j) + 16) & 31] = BLD(ro, vo_o, (32 * ((((j) + 16) & 31) >> 3) + ((j) & 7)) * (D * 2), 0)
  if (is_urg || is_mod) {
    const int mi = is_urg ? 4 * xe + hg : 8 * xe + 2 * hg + ((b >> 1) & 1);
    const __amdgpu_buffer_rsrc_t rm = MKRS(a.wmod, D * NMOD * 2);
    const int vo_m = trow * (NMOD * 2) + (is_urg ? 32 * mi + 8 * (ch8 & 3) + 512 * (ch8 >> 2) : 1024 + 64 * mi + 8 * ch8) * 2;
    const int ch = (lane >> 3) & 3, cc = 2 * cw + (lane >> 5);
    SB();
    BARRIER();
#pragma unroll
    for (int gr = 0; gr < 2; ++gr)
#pragma unroll
      for (int j = 0; j < 4; ++j) {
        const float a0 = lo2f(cf[gr][j]), a1 = hi2f(cf[gr][j]);
        cf[gr][j] = cvtpk(a0 * __builtin_amdgcn_rcpf(1.f + __expf(-a0)), a1 * __builtin_amdgcn_rcpf(1.f + __expf(-a1)));
      }
    PIN(cf[0]); PIN(cf[1]);
    {
      float xf[16];
#pragma unroll
      for (int j = 0; j < 4; ++j) { xf[2 * j] = lo2f(xr[j]); xf[2 * j + 1] = hi2f(xr[j]); xf[8 + 2 * j] = lo2f(xr2[j]); xf[9 + 2 * j] = hi2f(xr2[j]); }
      float s1 = 0.f;
#pragma unroll
      for (int j = 0; j < 16; ++j) s1 += xf[j];
      const float mean = half_allsum(s1) * (1.f / D);
      float s2 = 0.f;
#pragma unroll
      for (int j = 0; j < 16; ++j) { const float d = xf[j] - mean; s2 = fmaf(d, d, s2); }
      const float var = half_allsum(s2) * (1.f / D);
      if ((lane & 31) == 0) *reinterpret_cast<f32x2*>(s_ml + (2 * cw + (lane >> 5)) * 2) = f32x2{rbf(mean), rbf(sqrtf(rbf(rbf(var) + 1e-5f)))};
    }
    GET_TAG();
    SB();
    TILE_ISSUE(rm, vo_m, NMOD * 2);
    to_frags(wr); to_frags(wr + 8);
    TILE_MFMA(cf[0], cf[1]);
    if (is_urg) {
      const int oa = (cc * 8 + ch) * 8 + bq;
      float va = 0.f, vb = 0.f;
#pragma unroll
      for (int p = 0; p < 8; ++p) { va += s_mp[p * 512 + oa]; vb += s_mp[p * 512 + oa + 32]; }
      const f32x2 st = *reinterpret_cast<const f32x2*>(s_ml + bq * 2);
      PIN(xj);
      const float ln = rbf(rbf(((lane >> 5) ? hi2f(xj) : lo2f(xj)) - st[0]) / st[1]);
      const float hv = rbf(rbf(ln * rbf(1.f + rbf(vb))) + rbf(va));
      auto sw2 = __builtin_amdgcn_permlane32_swap(__float_as_uint(hv), __float_as_uint(hv), false, false);
      if (lane < 32) PUBLISH((sw2[0] >> 16) | (sw2[1] & 0xffff0000u), rw,
                             WS_HG + (((mi >> 1) * 2 + (cw >> 1)) * 64 + ch * 16 + (mi & 1) * 8 + bq) * 16 + (cw & 1) * 8, 0);
      STAMPM(3);
    } else {
      u32 pr;
      TILE_SUM2(4 * ((lane >> 3) & 1) + 2 * (lane & 1) + (lane >> 5), 4 * (g & 1) + ((lane >> 1) & 3), pr);
      PUBLISH(pr, rw, mg_off(2 * cw + (lane >> 5), mi >> 3) + ((mi & 7) * 32 + (lane & 31)) * 8, 0);
      STAMPM(7);
    }
  } else if (is_prod) {
    const int qi = 16 * xe + 4 * hg + (b - 4);
    const __amdgpu_buffer_rsrc_t rq = MKRS(a.wq, D * H * DH * 2);
    const int vo_q = trow * (H * DH * 2) + (64 * qi + 8 * ch8) * 2;
    BARRIER();
    TILE_ISSUE(rq, vo_q, H * DH * 2);
    GET_TAG();
    to_frags(wr); to_frags(wr + 8);
    SB();
#pragma unroll
    for (int j = 0; j < NQA; ++j) { WO_ISSUE(j); SB(); VMWAIT(PWA - 1); SB(); }
    WAIT_T(THP);
    const int vo_h = WS_HG + cw * 4096 + lane * 16;
    u32x4 hq[4];
    POLL_BEGIN()
#pragma unroll
      for (int i = 0; i < 4; ++i) hq[i] = BLD(rw, vo_h, i * 1024, 16);
      const bool ok = ok2(hq[0], th) & ok2(hq[1], th) & ok2(hq[2], th) & ok2(hq[3], th);
    POLL_END(ok)
    STAMPM(4);
    const u32x4 a0 = {hq[0][0], hq[0][2], hq[1][0], hq[1][2]}, a1 = {hq[2][0], hq[2][2], hq[3][0], hq[3][2]};
    TILE_MFMA(a0, a1);
    u32 pr;
    TILE_SUM2(2 * (lane & 3) + (lane >> 5), (lane >> 2) & 7, pr);
    PUBLISH(pr, rw, q_off(2 * cw + (lane >> 5), qi >> 1) + (32 * (qi & 1) + (lane & 31)) * 8, 0);
    STAMPM(5);
  } else {
    BARRIER();
    if (jj == JS && cw == 3) {
      u64 e_, c_;
      asm volatile("s_load_dwordx2 %0, %2, 0\n\ts_waitcnt vmcnt(%3)\n\ts_memrealtime %1\n\ts_waitcnt lgkmcnt(0)" : "=&s"(e_), "=&s"(c_) : "s"(a.ws + WS_EPOCH), "i"(5) : "memory");
      __builtin_amdgcn_raw_buffer_store_b64(u32x2{t0 + (((u32)c_ - t0) >> 1), (u32)(e_ + 1)}, rw, lane == 0 ? WS_RF + xcd * 8 : OOB, 0, 16);
    }
  }
  SB();
  asm volatile("" :: "v"(pf0), "v"(pf1), "v"(pfc), "v"(pf2));
  GET_TAG();
  LDS_ARRIVE(s_fl + 5);
  if (prb) {
    const unsigned char* pp = tw + T_PR + (hg * 4 + cw) * 128;
#pragma nounroll
    for (int p = 0; p < 8 * NPROBE; ++p) {
      const int k = (p + 2 * cw + hg) & 7;
      u64 c0, c1; u32 z;
      asm volatile("s_memtime %0\n\ts_load_dword %2, %3, %4\n\ts_waitcnt lgkmcnt(0)\n\ts_memtime %1\n\ts_waitcnt lgkmcnt(0)"
                   : "=&s"(c0), "=&s"(c1), "=&s"(z) : "s"(pp), "s"(k * 4096 + (p >> 3) * 2048) : "memory");
      if (lane == 0) __hip_atomic_fetch_max(s_fl + 16 + k, 4095u - min((u32)c1 - (u32)c0, 4095u), RLX_WG);
    }
    LDS_ARRIVE(s_fl + 10);
    if (cw == 3) {
      LDS_SPIN(s_fl + 10, 4u);
      const u32x4 ma = *reinterpret_cast<const u32x4*>(s_fl + 16), mb = *reinterpret_cast<const u32x4*>(s_fl + 20), mc = ma + mb, md = mb - ma;
#define VOTE(x) (u32)min(max(((int)(x) >> 3) + 128, 0), 255)
      PUBLISH(VOTE(mc[1] + mc[2] - mc[0] - mc[3]) | VOTE(mc[2] + mc[3] - mc[0] - mc[1]) << 10 | VOTE(md[0] + md[1] + md[2] + md[3]) << 20, rt, lane < 8 ? T_CP + lane * 128 + hg * 32 : OOB, 0);
      STAMP(19);
    }
  }
  const int nwa = prb ? (cw ? NWE : NWM0) : is_prod ? (cw ? NWQ : NQA) : has_tile ? (cw ? NWM : NWM0) : (cw ? NWP : NWP0);
#pragma unroll
  for (int j = 0; j < NWP; ++j) {
    if (j < nwa && (j >= NQA || !is_prod)) { WO_ISSUE(j); SB(); VMWAIT(PWA - 1); SB(); }
  }
  if (cw == 0) {
    STAMP(22);
    WAIT_T(TQP);
    u32x2 qg;
    POLL_BEGIN()
      qg = BLD64(rw, q_off(bd, h) + lane * 8, 0, 16);
      const bool ok = qg[1] == th;
      __builtin_amdgcn_s_sleep(4);
    POLL_END(ok)
    *reinterpret_cast<u32*>(lds + L_Q + lane * 4) = qg[0];
    STAMP(6);
    LDS_ARRIVE(s_fl + 6);
  }
  if (PADC) asm volatile("s_branch 1f\n\t.fill %0, 4, 0xBF800000\n1:" :: "i"(PADC / 4 - 1));
  LDS_SPIN(s_fl + 6, 1u);
  LDS_SPIN(s_fl + 5, 8u);
  {
    u32x4 qa[4];
#pragma unroll
    for (int j = 0; j < 4; ++j) qa[j] = *reinterpret_cast<const u32x4*>(lds + L_Q + j * 64 + g * 16);
    f32x4 acc[8];
#pragma unroll
    for (int T = 0; T < 8; ++T) {
      acc[T] = f32x4{0.f, 0.f, 0.f, 0.f};
#pragma unroll
      for (int j = 0; j < 4; ++j) {
        const u32x4 kf = *reinterpret_cast<const u32x4*>(lds + L_K + (128 * cw + n) * 256 + (((4 * j + g) ^ n) << 4) + T * 4096);
        acc[T] = mfma(qa[j], kf, acc[T]);
      }
    }
    const float s0 = rbf(rbf(sel4(acc[0][0], acc[1][0], acc[2][0], acc[3][0], g)) * SCALE);
    const float s1 = rbf(rbf(sel4(acc[4][0], acc[5][0], acc[6][0], acc[7][0], g)) * SCALE);
    s_pt[n * 68 + 8 * cw + g] = s0;
    s_pt[n * 68 + 8 * cw + 4 + g] = s1;
    const float mw = wave_allmax(fmaxf(s0, s1));
    const float lw = wave_allsum(__expf(s0 - mw) + __expf(s1 - mw));
    if (lane == 0) *reinterpret_cast<f32x2*>(s_ml + wave * 2) = f32x2{mw, lw};
  }
  LDS_ARRIVE(s_fl + 7);
  if (cw == 3) {
    u32x2 va;
    POLL_BEGIN()
      va = BLD64(rt, T_CP + xcd * 128 + (lane & 3) * 32, 0, 16);
      const bool ok = va[1] == th;
      __builtin_amdgcn_s_sleep(4);
    POLL_END(ok)
    u32 vs = va[0] + (u32)__builtin_amdgcn_mov_dpp((int)va[0], XOR1, 0xf, 0xf, true);
    vs += (u32)__builtin_amdgcn_mov_dpp((int)vs, XOR2, 0xf, 0xf, true);
    if (lane == 0) __hip_atomic_store(s_fl + 9, ((((vs << 3) ^ (vs >> 7)) & 4096u) | ((vs >> 6) & 8192u) | ((vs >> 15) & 16384u)) ^ 0x3001u, RLX_WG);
  }
  u32x8 wf[16];
  int cp;
  u32 zs;
  const int lvl = __builtin_amdgcn_readfirstlane(nwa >= 18 ? 0 : nwa >= 10 ? 1 : 2);
  int nwb = __builtin_amdgcn_readfirstlane(nwa);
  asm volatile("" : "+s"(nwb));
  {
#pragma unroll
    for (int j = 0; j <= JD0; ++j) {
      if (j == (PSJ < JD0 ? PSJ : JD0)) {
        LDS_SPIN(s_fl + 9, 1u);
        cp = __builtin_amdgcn_readfirstlane(s_fl[9]) & 0x7000;
        const int c_ = cls(xcd) ^ cp;
        asm volatile("s_load_dword %0, %1, 0\n\ts_load_dword %0, %1, 64\n\ts_load_dword %0, %1, 128\n\ts_load_dword %0, %1, 192\n\t"
                     "s_load_dword %0, %1, 256\n\ts_load_dword %0, %1, 320\n\ts_load_dword %0, %1, 384\n\ts_load_dword %0, %1, 448\n\t"
                     "s_load_dword %0, %2, 0\n\ts_load_dword %0, %2, 64\n\ts_load_dword %0, %2, 128\n\ts_load_dword %0, %3, 0\n\ts_load_dword %0, %3, 64"
                     : "=&s"(zs) : "s"(tw + (T_IN + (jj >> 1) * 32768 + c_ + (jj & 1) * 2048 + cw * 512)),
                       "s"(tw + (T_INX + (jj >> 2) * 32768 + (c_ ^ 4096) + (jj & 3) * 1024 + cw * 192)),
                       "s"(tw + (T_X1 + (jj >> 3) * 32768 + (c_ ^ 16384) + (jj & 7) * 512 + cw * 128)) : "memory");
        SB();
      }
      if (j < JD0 && j >= nwb) {
        WO_ISSUE(j); SB();
        asm volatile("s_waitcnt vmcnt(%3)\n\ts_cmp_gt_i32 %0, 1\n\ts_cbranch_scc1 1f\n\ts_waitcnt vmcnt(%2)\n\ts_cmp_gt_i32 %0, 0\n\ts_cbranch_scc1 1f\n\ts_waitcnt vmcnt(%1)\n1:"
                     :: "s"(lvl), "i"(PB0 - 1), "i"(PB1 - 1), "i"(PB2 - 1) : "memory", "scc");
        SB();
      }
    }
  }
  to_frags2(wr + 16, wf + 8);
#define FRAG2(j) do { _Pragma("unroll") for (int c = 0; c < 8; ++c) { \
    u32 o_ = (c & 1) ? perm_hi(wr[2 * (j)][c >> 1], wr[2 * (j) + 1][c >> 1]) : perm_lo(wr[2 * (j)][c >> 1], wr[2 * (j) + 1][c >> 1]); wf[c][j] = o_; } } while (0)
#pragma unroll
  for (int j = 0; j < (JD0 - 16) / 2; ++j) FRAG2(j);
#pragma unroll
  for (int c = 0; c < 8; ++c) PIN(wf[c]);
  SB();
  asm volatile("s_waitcnt lgkmcnt(0)" : "+s"(zs) :: "memory");
  float e0[8];
  float m0 = 0.f;
  const int rcol = 32 * (rc >> 1) + 4 * (rc & 1) + 8 * (bq >> 1) + 2 * (bq & 1);
  u32 a_h2w = LADDR(L_H2 + cw * HROW + (lane >> 2) * 32 + (lane & 3) * 4);
  if (is_red) {
    const u32 xp = *reinterpret_cast<const u32*>(a.x + rb * D + rcol);
    const int vo_m = mg_off(rb, 0) + (rc * 8 + bq) * 8;
    u32x2 ga, gb;
    POLL_BEGIN()
      ga = BLD64(rw, vo_m, 0, 16); gb = BLD64(rw, vo_m, 3 * 2048, 16);
      const bool ok = (ga[1] == th) & (gb[1] == th);
    POLL_END(ok)
    e0[0] = lo2f(xp); e0[1] = hi2f(xp); e0[2] = lo2f(ga[0]); e0[3] = hi2f(ga[0]); e0[4] = lo2f(gb[0]); e0[5] = hi2f(gb[0]);
#pragma unroll
    for (int i = 0; i < 6; ++i) PIN(e0[i]);
  } else if (is_unit) {
    {
      const int vo_m = mg_off(ub + cw, 0) + lane * 16;
      const u32x4 x0 = *reinterpret_cast<const u32x4*>(a.x + (ub + cw) * D + lane * 8);
      u32x4 m4[4];
      POLL_BEGIN()
#pragma unroll
        for (int i = 0; i < 4; ++i) m4[i] = BLD(rw, vo_m, 2048 + i * 1024, 16);
        const bool ok = ok2(m4[0], th) & ok2(m4[1], th) & ok2(m4[2], th) & ok2(m4[3], th);
      POLL_END(ok)
#pragma unroll
      for (int i = 0; i < 4; ++i) {
        const u32 cbp = m4[2 + (i >> 1)][2 * (i & 1)], c1 = cvtpk(1.f + lo2f(cbp), 1.f + hi2f(cbp));
        *LPTR(u32, a_h2w + (4 + cw) * HROW + (i >> 1) * 512 + (i & 1) * 16) = c1;
        *LPTR(u32, a_h2w + (5 + cw) * HROW + (i >> 1) * 512 + (i & 1) * 16) = m4[i >> 1][2 * (i & 1)];
        e0[2 * i] = lo2f(c1); e0[2 * i + 1] = hi2f(c1);
      }
      float s0 = 0.f;
#pragma unroll
      for (int i = 0; i < 4; ++i) s0 += lo2f(x0[i]) + hi2f(x0[i]);
      m0 = wave_allsum(s0) * (1.f / D);
#pragma unroll
      for (int i = 0; i < 8; ++i) PIN(e0[i]);
      PIN(m0);
      *LPTR(float, LADDR(L_H2 + 1024) + cw * HROW) = m0;
    }
  }
  u32 a_of = LADDR(L_OP + (2 * ((n >> 2) & 1) + (n & 1)) * OROW + g * 32 + (n & 2) * 8 + (n >> 3) * 2048);
  u32 a_og = a_of + ((n >> 3) ? -2048 : 2048);
  int sidx = (n & 2) ? 0xEEEE : 0x4444;
  f32x4 acc[4];
#pragma unroll
  for (int c = 0; c < 4; ++c) { acc[c] = f32x4{0.f, 0.f, 0.f, 0.f}; PIN(acc[c]); }
  PIN(sidx);
  int vo2 = (in_off(bd, 8 * cw + 2 * (n >> 2) + (g >> 1)) ^ cp) + h * 64 + (n & 3) * 16;
  u32 thv = th;
  u32x4 pv = {0u, th, 0u, th};
  u64 evn = 0x0000ffff0000ffffull;
  asm volatile("" : "+s"(evn));
  PIN(a_og); PIN(pv);
  PIN(a_of); PIN(a_h2w); PIN(vo2); PIN(thv);
  asm volatile("" :: "s"(rt));
  const int pcofs = min(cg0 * 32 + min(lane & 3, ncg - 3) * 64, F - 2);
  u32 pfa = (16 * jj + (lane >> 2)) * F + pcofs;
  PIN(pfa);
  u64 pfs = (u64)(uintptr_t)(cw == 0 ? a.wg : cw == 1 ? a.wu : a.wo + (size_t)h * DH * D);
  asm volatile("" : "+s"(pfs));
  u32 pfo = (cw < 2 ? pfa : 0u) * 2u;
  PIN(pfo);
  {
    u32 a_pr = LADDR(L_PG + cw * 256);
    PIN(a_pr);
    const int lim = 2 * TSTAR - 64 * TAU;
    int p = 0;
#pragma unroll
    for (int j = JD0; j < 32; ++j) {
      u32 tn, tv; int ta;
      asm volatile("s_cmp_gt_i32 %0, 63\n\ts_cbranch_scc1 3f\n"
                   "1:\n\t"
                   "s_memrealtime vcc\n\tds_read_b32 %2, %4\n\ts_waitcnt lgkmcnt(0)\n\t"
                   "v_readfirstlane_b32 %0, %2\n\ts_sub_u32 %3, vcc_lo, %5\n\ts_lshl_b32 %3, %3, 1\n\t"
                   "s_cmp_gt_i32 %0, 63\n\ts_cbranch_scc1 3f\n\t"
                   "s_cmp_lt_i32 %0, %7\n\ts_cbranch_scc1 2f\n\t"
                   "s_mul_i32 %1, %0, %8\n\ts_sub_i32 %3, %3, %1\n\t"
                   "s_cmp_lt_i32 %3, %6\n\ts_cbranch_scc1 3f\n"
                   "2:\n\ts_sleep 2\n\ts_branch 1b\n"
                   "3:"
                   : "+s"(p), "=&s"(tn), "=&v"(tv), "=&s"(ta) : "v"(a_pr), "s"(t0), "s"(lim), "i"(PK0 + PKS * (j - JD0)), "i"(TAU) : "memory", "scc", "vcc");
      SB();
      WO_ISSUE(j); SB();
      SB();
    }
  }
  LDS_ARRIVE(s_fl + 13);
  SB();
  LDS_SPIN(s_fl + 13, 8u);
  const u32 pfm = *reinterpret_cast<const GAS u32*>((const GAS unsigned char*)pfs + pfo);
  SB();
  STAMPM(21);
  if (cw == 0) STAMP(8);
  BARRIER();
  if (cw == 0) STAMP(9);
  {
    const u32x4 af1 = *LPTR(const u32x4, a_of + 128), ag1 = *LPTR(const u32x4, a_og + 128), af0 = *LPTR(const u32x4, a_of), ag0 = *LPTR(const u32x4, a_og);
    SB();
#define FRAGP(j, c0, c1) do { _Pragma("unroll") for (int c = (c0); c < (c1); ++c) { \
    u32 o_ = (c & 1) ? perm_hi(wr[2 * (j)][c >> 1], wr[2 * (j) + 1][c >> 1]) : perm_lo(wr[2 * (j)][c >> 1], wr[2 * (j) + 1][c >> 1]); wf[c][j] = o_; PIN(wf[c]); } SB(); } while (0)
#pragma unroll
    for (int j = (JD0 - 16) / 2; j < 6; ++j) FRAGP(j, 0, 8);
    float r[4];
    f32x2 tf[4];
#define FOLD0(c) do { tf[c] = fold(acc[c]); SB(); } while (0)
#define FOLD1(c) do { tf[c] += f32x2{tf[c][1], tf[c][0]}; PIN(tf[c]); SB(); } while (0)
#define FOLD2(c) do { r[c] = swap16_add(tf[c][0], tf[c][1]); PIN(r[c]); SB(); } while (0)
#pragma unroll
    for (int i = 0; i < 16; ++i) {
      const int c = (i >> 1) & 3, k = 2 * (i & 1) + (i < 8);
      acc[c] = smfmac(k == 0 ? af0 : k == 1 ? af1 : k == 2 ? ag0 : ag1, wf[8 * (k & 1) + 4 * (k >> 1) + c], acc[c], sidx);
      SB();
      if (i >= 1 && i < 4) FRAGP(6, 3 * (i - 1), i == 3 ? 8 : 3 * i);
      if (i >= 5 && i < 8) FRAGP(7, 3 * (i - 5), i == 7 ? 8 : 3 * (i - 4));
      if (i == 12) { FOLD0(0); FOLD1(0); }
      if (i == 13) FOLD2(0);
      if (i == 14) { FOLD0(1); FOLD1(1); }
    }
    FOLD2(1);
    FOLD0(2);
    pv[0] = cvtpk(r[0], r[1]); PIN(pv);
    SB();
    FOLD1(2); FOLD2(2);
    FOLD0(3); FOLD1(3);
    pv[2] = cvtpk(r[2], swap16_add(tf[3][0], tf[3][1]));
    asm volatile("s_mov_b64 exec, %3\n\tbuffer_store_dwordx4 %0, %1, %2, 0 offen sc1\n\ts_mov_b64 exec, -1\n\ts_nop 0" :: "v"(pv), "v"(vo2), "s"(rt), "s"(evn) : "memory");
  }
  SB();
  if (is_unit) __builtin_amdgcn_s_barrier();
  SB();
  if (cw == 0) STAMP(10);
  const u32 th2 = th ^ 0x80000000u;
  if (is_red) {
    int vi = (in_off(rb, rc) ^ cp) + lane * 8, vx1 = (x1_off(lane >> 3, rb) ^ cp) + (rc * 8 + bq) * 8;
    const u32 s1_ = (n & 2) ? 0u : (n & 1) ? 0x3f800000u : 0x00003f80u;
    u32x4 sel = {s1_, s1_, s1_, s1_};
    int s4 = 0x4444;
    PIN(vi); PIN(vx1); PIN(sel); PIN(s4);
    __builtin_amdgcn_s_sleep(D2);
    u32x8 eg[2];
    f32x4 ya;
    POLL_BEGIN()
#pragma unroll
      for (int i = 0; i < 4; ++i) { const u32x2 v_ = BLD64(rt, vi, i * 512, 16); eg[0][2 * i] = v_[0]; eg[0][2 * i + 1] = v_[1]; }
      ya = f32x4{0.f, 0.f, 0.f, 0.f};
      PIN(ya);
      SB();
      u32 k0 = ne3(eg[0][1], eg[0][3], th);
      PIN(k0);
      SB();
      ya = smfmac(sel, eg[0], ya, s4);
      SB();
      const bool ok = (k0 | ne3(eg[0][5], eg[0][7], th)) == 0u;
    POLL_END(ok)
    STAMPM(11);
    asm volatile("" :: "v"(pfm));
    float y0 = ya[0] + dppf<XOR8>(ya[0]);
    PIN(y0);
    float y1 = ya[1] + dppf<XOR8>(ya[1]);
    PIN(y1);
    y0 = fmaf(e0[2], y0, e0[0]);
    PIN(y0);
    const u32 x1p = cvtpk(y0, fmaf(e0[3], y1, e0[1]));
    __builtin_amdgcn_raw_buffer_store_b64(u32x2{x1p, thv}, rt, vx1, 0, 16);
    SB();
    STAMPM(12);
    const bool pad = lane >= 24;
    const __amdgpu_buffer_rsrc_t rout = MKRS(a.out, B * D * 2);
    int vout = lane < 8 ? (rb * D + rcol) * 2 : OOB;
    float x1a = lo2f(x1p), x1b = hi2f(x1p);
    int u4 = (inx_off(rb, rc) ^ cp) + lane * 8, u5 = pad ? OOB : u4;
    u32 tp = pad ? th2 : 0u;
    PIN(vout); PIN(x1a); PIN(x1b); PIN(u4); PIN(u5); PIN(tp);
    asm volatile("" :: "s"(rout));
    __builtin_amdgcn_s_sleep(D5);
    POLL_BEGIN()
#pragma unroll
      for (int i = 0; i < 6; ++i) { const u32x2 v_ = BLD64(rt, i < 4 ? vi : i < 5 ? u4 : u5, (i & 3) * 512, 16); eg[i >> 2][2 * (i & 3)] = v_[0]; eg[i >> 2][2 * (i & 3) + 1] = v_[1]; }
#pragma unroll
      for (int i = 4; i < 8; ++i) eg[1][i] = 0u;
      ya = f32x4{0.f, 0.f, 0.f, 0.f};
      PIN(ya);
      SB();
      ya = smfmac(sel, eg[0], ya, s4);
      SB();
      u32 k0 = ne3(eg[0][1], eg[0][3], th2) | ne3(eg[0][5], eg[0][7], th2);
      PIN(k0);
      SB();
      ya = smfmac(sel, eg[1], ya, s4);
      SB();
      const bool ok = (k0 | ne3(eg[1][1], eg[1][3] | tp, th2)) == 0u;
    POLL_END(ok)
    STAMPM(13);
    y0 = ya[0] + dppf<XOR8>(ya[0]);
    PIN(y0);
    y1 = ya[1] + dppf<XOR8>(ya[1]);
    PIN(y1);
    y0 = fmaf(e0[4], y0, x1a);
    PIN(y0);
    __builtin_amdgcn_raw_buffer_store_b32(cvtpk(y0, fmaf(e0[5], y1, x1b)), rout, vout, 0, 17);
    STAMPM(14);
    STAMP_DUMP();
    asm volatile("s_endpgm\n\t.fill %0, 4, 0xBF800000" :: "i"(PADB / 4) : "memory");
    return;
  }
  if (!is_unit) {
    if (b >= 3) {
      const int li = min(((hg * 3 + b - 3) * 4 + cw) * 32 + (lane & 31), ncg * 256 - 1);
      const u32 pd_ = *reinterpret_cast<const GAS u32*>((const GAS unsigned char*)a.wd + (u32)(cg0 * 256 + li) * 128u);
      asm volatile("" :: "v"(pd_));
    }
    asm volatile("" :: "v"(pfm));
    if (w == EPW && cw == 3) {
      POLL_BEGIN()
        const bool ok = BLD64(rt, (x1_off(xcd, (lane >> 2) & 7) ^ cp) + (lane & 3) * 512, 0, 16)[1] == th;
        __builtin_amdgcn_s_sleep(24);
      POLL_END(ok)
      if (lane == 0) __hip_atomic_store((GAS u64*)(a.ws + WS_EPOCH), E + 1, RLX_AGENT);
    }
    EXIT();
  }
  const __amdgpu_buffer_rsrc_t rd = MKRS(a.wd + (size_t)cg * 32 * D, 32 * D * 2);
  const int vo_d = g * 8 * (D * 2) + cw * 256 + n * 16;
  const int r4 = 8 * cw + 2 * (n >> 2);
  int vo4 = ((cg < 32 ? in_off(ub + g, r4) : inx_off(ub + g, r4)) ^ cp) + ((cg & 31) ^ (cg < 40 ? 2 * (n >> 2) : 0)) * 64 + (n & 3) * 16;
  u32 a_hr = LADDR((g >> 1) == (n & 1) ? L_HH + (n >> 2) * 64 + (g & 1) * 32 + (n & 2) * 8 : L_FL + 96);
  f32x4 ad[4];
#pragma unroll
  for (int f = 0; f < 4; ++f) { ad[f] = f32x4{0.f, 0.f, 0.f, 0.f}; PIN(ad[f]); }
  u32x4 pw[2] = {{0u, th2, 0u, th2}, {0u, th2, 0u, th2}};
  int vo5 = vo4 ^ 16384;
  PIN(vo4); PIN(vo5); PIN(a_hr); PIN(pw[0]); PIN(pw[1]);
  u32x4 dr[8];
  u32x8 df[4];
  __builtin_amdgcn_s_barrier();
  SB();
#pragma unroll
  for (int e = 0; e < 8; ++e) { dr[e] = BLD(rd, vo_d, e * (D * 2), 0); SB(); }
#define DN_FRAGS() do { _Pragma("unroll") for (int f = 0; f < 4; ++f) \
    _Pragma("unroll") for (int j = 0; j < 4; ++j) { df[f][j] = perm_lo(dr[2 * j][f], dr[2 * j + 1][f]); df[f][4 + j] = perm_hi(dr[2 * j][f], dr[2 * j + 1][f]); } \
    _Pragma("unroll") for (int f = 0; f < 4; ++f) PIN(df[f]); } while (0)
  {
    int vx = (x1_off(xcd, ub + cw) ^ cp) + lane * 16;
    u32 a_sw = LADDR(L_PG + cw * 256 + n * 16 + (g & 1) * 8 + (g >> 1) * 4);
    f32x2 e1[4];
#pragma unroll
    for (int i = 0; i < 4; ++i) { e1[i] = f32x2{e0[2 * i], e0[2 * i + 1]} * -m0; PIN(e1[i]); }
    float s1 = 0.f, s2 = 0.f;
    PIN(vx); PIN(a_sw); PIN(s1); PIN(s2);
    u32x4 xa, xb;
    xa = BLD(rt, vx, 0, 16); xb = BLD(rt, vx, 1024, 16);
    SB();
    DN_FRAGS();
    SB();
    if (__builtin_amdgcn_ballot_w64((ne3(xa[1], xa[3], th) | ne3(xb[1], xb[3], th)) != 0u) != 0ull) {
      POLL_BEGIN()
        xa = BLD(rt, vx, 0, 16); xb = BLD(rt, vx, 1024, 16);
        const bool ok = (ne3(xa[1], xa[3], th) | ne3(xb[1], xb[3], th)) == 0u;
      POLL_END(ok)
    }
    if (cw == 1) STAMP(15);
    const u32 d[4] = {xa[0], xa[2], xb[0], xb[2]};
    f32x2 zz[4];
#pragma unroll
    for (int i = 0; i < 4; ++i) zz[i] = f32x2{lo2f(d[i]), hi2f(d[i])};
    SB();
#pragma unroll
    for (int i = 0; i < 4; ++i) zz[i] = __builtin_elementwise_fma(zz[i], f32x2{e0[2 * i], e0[2 * i + 1]}, e1[i]);
    SB();
    *LPTR(u32, a_h2w) = cvtpk(zz[0][0], zz[0][1]); *LPTR(u32, a_h2w + 16) = cvtpk(zz[1][0], zz[1][1]);
    *LPTR(u32, a_h2w + 512) = cvtpk(zz[2][0], zz[2][1]); *LPTR(u32, a_h2w + 528) = cvtpk(zz[3][0], zz[3][1]);
    SB();
#pragma unroll
    for (int i = 0; i < 4; ++i) { s1 = dot2(d[i], 0x3f803f80u, s1); s2 = dot2(d[i], d[i], s2); }
    PIN(s1); PIN(s2);
    float r = swap32_add(s1, s2);
    r += dppf<XOR1>(r);
    r += dppf<XOR2>(r);
    PIN(r);
    BARRIER();
    r += dppf<XOR7>(r);
    r += dppf<XOR15>(r);
    *LPTR(float, a_sw) = r;
    SB();
    BARRIER();
  }
  asm volatile("" :: "v"(pfm));
  BARRIER();
  {
    const u32x4 af = *LPTR(const u32x4, a_hr);
#pragma unroll
    for (int f = 0; f < 4; ++f) ad[f] = smfmac(af, df[f], ad[f], sidx);
    SB();
#pragma unroll
    for (int f = 0; f < 2; ++f) {
      const f32x2 va = fold(ad[2 * f]), vb = fold(ad[2 * f + 1]);
      pw[f][0] = cvtpk(va[0], va[1]); pw[f][2] = cvtpk(vb[0], vb[1]);
      __builtin_amdgcn_raw_buffer_store_b128(pw[f], rt, f ? vo5 : vo4, 0, 16);
      SB();
    }
  }
  if (cw == 0) STAMP(26);
  EXIT();
}

extern "C" void kernel_launch(void* const* d_in, const int* in_sizes, int n_in, void* d_out, int out_size, void* d_ws, size_t ws_size,
                              hipStream_t stream) {
  Args a;
  a.x = (const u16*)d_in[0]; a.kc = (const u16*)d_in[1]; a.vc = (const u16*)d_in[2]; a.wq = (const u16*)d_in[3]; a.wo = (const u16*)d_in[4];
  a.wg = (const u16*)d_in[6]; a.wu = (const u16*)d_in[7]; a.wd = (const u16*)d_in[8]; a.c = (const u16*)d_in[9]; a.wmod = (const u16*)d_in[10];
  a.out = (u16*)d_out; a.ws = (unsigned char*)d_ws;
  k_block<<<256, 512, 0, stream>>>(a);
}
```
